```python
import torch

def _softmax(x, axis=-1):
    return torch.softmax(x, dim=axis)
def _gelu(x):
    return torch.nn.functional.gelu(x, approximate="tanh")
def _silu(x):
    return torch.nn.functional.silu(x)
def _sigmoid(x):
    return torch.sigmoid(x)
def _layernorm(x, axis=-1, eps=1e-5):
    m = x.mean(dim=axis, keepdim=True)
    v = x.var(dim=axis, keepdim=True, unbiased=False)
    return (x - m) / torch.sqrt(v + eps)
def _scatter_add(tgt, idx, src, axis=0):
    return tgt.clone().index_add_(axis, idx, src)
def _segment_sum(src, seg, n):
    out = torch.zeros((n,) + src.shape[1:], dtype=src.dtype)
    return out.index_add_(0, seg, src)
def _pad(x, pw):
    flat = tuple(v for pair in reversed(pw) for v in pair)
    return torch.nn.functional.pad(x, flat)

kernel_name = 'concat_heads__grouped_kv_mqa__spectral_gfnet__prompt'

def setup_inputs():

    _w = (lambda a: (lambda t: t.to(torch.bfloat16) if t.is_floating_point() else t)(torch.as_tensor(a)))
    def _A_setup():
        B, S, D, H = 1, 2048, 576, 32
        Dh = D // H
        return {
            "x": _w((lambda *s: torch.randn(*s, dtype=torch.bfloat16) * 0.05)(B, S, D)),
            "wq": _w((lambda *s: torch.randn(*s, dtype=torch.bfloat16) * 0.8)(D, H * Dh)),
            "wk": _w((lambda *s: torch.randn(*s, dtype=torch.bfloat16) * 0.8)(D, Dh)),
            "wv": _w((lambda *s: torch.randn(*s, dtype=torch.bfloat16) * 0.05)(D, Dh)),
            "wo": _w((lambda *s: torch.randn(*s, dtype=torch.bfloat16) * 0.05)(H * Dh, D)),
        }
    _A_in = _A_setup()

    def _B_setup():
        B, S, D = 1, 2048, 576
        Df = D // 2 + 1
        return {
            "x": _w((lambda *s: torch.randn(*s, dtype=torch.bfloat16) * 0.05)(B, S, D)),
            "w_r": _w((lambda *s: torch.randn(*s, dtype=torch.bfloat16) * 0.8)(S, Df) * 0.02),
            "w_i": _w((lambda *s: torch.randn(*s, dtype=torch.bfloat16) * 0.8)(S, Df) * 0.02),
        }
    _B_in = _B_setup()

    return {
        "A_x": _A_in["x"],
        "A_wq": _A_in["wq"],
        "A_wk": _A_in["wk"],
        "A_wv": _A_in["wv"],
        "A_wo": _A_in["wo"],
        "B_x": _B_in["x"],
        "B_w_r": _B_in["w_r"],
        "B_w_i": _B_in["w_i"],
    }

def reference(A_x, A_wq, A_wk, A_wv, A_wo, B_x, B_w_r, B_w_i):
    def _A_ref(x, wq, wk, wv, wo):
        B, S, D = x.shape
        H = 32
        Dh = D // H
        q = (lambda x, *d: x.permute(*d))((x @ wq).reshape(B, S, H, Dh), 0, 2, 1, 3)
        k = x @ wk
        v = x @ wv
        scores = torch.einsum("bhqd,bkd->bhqk", q, k) * (Dh ** -0.5)
        p = _softmax(scores, axis=-1)
        o = torch.einsum("bhqk,bkd->bhqd", p, v)
        return (lambda x, *d: x.permute(*d))(o, 0, 2, 1, 3).reshape(B, S, H * Dh) @ wo

    def _B_ref(x, w_r, w_i):
        B, S, D = x.shape
        X = (lambda x, axes=(-2,-1): torch.fft.rfft2(x.float(), dim=axes))(x, axes=(-2, -1))
        W = w_r + 1j * w_i
        X = X * W
        y = (lambda x, s, axes=(-2,-1): torch.fft.irfft2(x, s=s, dim=axes).to(torch.bfloat16))(X, (S, D), axes=(-2, -1))
        return y

    a = _A_ref(A_x, A_wq, A_wk, A_wv, A_wo)
    b = _B_ref(B_x, B_w_r, B_w_i)
    return torch.concatenate([a, b], axis=-1)
```

```cpp
#include <hip/hip_runtime.h>
#include <hip/hip_bf16.h>
#include <stdint.h>

namespace {
constexpr int S = 2048, D = 576, H = 32, DH = 18, DF = 289;
typedef unsigned short u16;
__device__ __forceinline__ float b2f(u16 v) { return __uint_as_float(((unsigned)v) << 16); }
__device__ __forceinline__ u16 f2b(float f) { __bf16 b = (__bf16)f; return __builtin_bit_cast(u16, b); }

__global__ void k_gemm(const u16* __restrict__ A, const u16* __restrict__ B, u16* __restrict__ C, int M, int N, int K, int ldc) {
  int n = blockIdx.x * blockDim.x + threadIdx.x, m = blockIdx.y;
  if (n >= N) return;
  float acc = 0.f;
  for (int k = 0; k < K; ++k) acc += b2f(A[m * K + k]) * b2f(B[k * N + n]);
  C[m * ldc + n] = f2b(acc);
}
__global__ void k_attn(const u16* __restrict__ q, const u16* __restrict__ k, const u16* __restrict__ v, u16* __restrict__ o) {
  int idx = blockIdx.x * blockDim.x + threadIdx.x;
  int t = idx / H, h = idx % H;
  float qv[DH], acc[DH];
  for (int d = 0; d < DH; ++d) { qv[d] = b2f(q[t * D + h * DH + d]); acc[d] = 0.f; }
  float m = -INFINITY, l = 0.f;
  const float sc = 0.23570226039551584f;
  for (int j = 0; j < S; ++j) {
    float s = 0.f;
    for (int d = 0; d < DH; ++d) s += qv[d] * b2f(k[j * DH + d]);
    s *= sc;
    float mn = fmaxf(m, s), a = __expf(m - mn), p = __expf(s - mn);
    l = l * a + p;
    for (int d = 0; d < DH; ++d) acc[d] = acc[d] * a + p * b2f(v[j * DH + d]);
    m = mn;
  }
  for (int d = 0; d < DH; ++d) o[t * D + h * DH + d] = f2b(acc[d] / l);
}
__global__ void k_rowdft(const u16* __restrict__ x, float2* __restrict__ X1) {
  int f = blockIdx.x * blockDim.x + threadIdx.x, s = blockIdx.y;
  if (f >= DF) return;
  float re = 0.f, im = 0.f;
  for (int d = 0; d < D; ++d) { int a = (f * d) % D; float sn, cs; sincospif(-2.0f * a / D, &sn, &cs); float xv = b2f(x[s * D + d]); re += xv * cs; im += xv * sn; }
  X1[s * DF + f] = make_float2(re, im);
}
__global__ void k_coldft(const float2* __restrict__ X, float2* __restrict__ Y, int sign, const u16* wr, const u16* wi) {
  int f = blockIdx.x * blockDim.x + threadIdx.x, k = blockIdx.y;
  if (f >= DF) return;
  float re = 0.f, im = 0.f;
  for (int s = 0; s < S; ++s) { int a = (k * s) & (S - 1); float sn, cs; sincospif(sign * 2.0f * a / S, &sn, &cs); float2 xv = X[s * DF + f]; re += xv.x * cs - xv.y * sn; im += xv.x * sn + xv.y * cs; }
  if (wr) { float a = b2f(wr[k * DF + f]), b = b2f(wi[k * DF + f]); float r2 = re * a - im * b, i2 = re * b + im * a; re = r2; im = i2; }
  Y[k * DF + f] = make_float2(re, im);
}
__global__ void k_rowidft(const float2* __restrict__ Z, u16* __restrict__ out) {
  int d = blockIdx.x * blockDim.x + threadIdx.x, s = blockIdx.y;
  if (d >= D) return;
  float acc = 0.f;
  for (int f = 0; f < DF; ++f) { int a = (f * d) % D; float sn, cs; sincospif(2.0f * a / D, &sn, &cs); float2 z = Z[s * DF + f]; float w = (f == 0 || f == D / 2) ? 1.f : 2.f;
    float t = (f == 0 || f == D / 2) ? z.x * cs : z.x * cs - z.y * sn; acc += w * t; }
  out[s * (2 * D) + D + d] = f2b(acc * (1.0f / (S * D)));
}
}

extern "C" void kernel_launch(void* const* d_in, const int* in_sizes, int n_in, void* d_out, int out_size, void* d_ws, size_t ws_size, hipStream_t stream) {
  const u16* ax = (const u16*)d_in[0]; const u16* wq = (const u16*)d_in[1]; const u16* wk = (const u16*)d_in[2]; const u16* wv = (const u16*)d_in[3]; const u16* wo = (const u16*)d_in[4];
  const u16* bx = (const u16*)d_in[5]; const u16* wr = (const u16*)d_in[6]; const u16* wi = (const u16*)d_in[7];
  u16* out = (u16*)d_out; char* ws = (char*)d_ws;
  u16* q = (u16*)ws; u16* k = (u16*)(ws + (4 << 20)); u16* v = (u16*)(ws + (5 << 20)); u16* o = (u16*)(ws + (6 << 20));
  float2* X1 = (float2*)(ws + (16 << 20)); float2* X2 = (float2*)(ws + (32 << 20)); float2* X3 = (float2*)(ws + (48 << 20));
  k_gemm<<<dim3(3, S), 192, 0, stream>>>(ax, wq, q, S, D, D, D);
  k_gemm<<<dim3(1, S), 64, 0, stream>>>(ax, wk, k, S, DH, D, DH);
  k_gemm<<<dim3(1, S), 64, 0, stream>>>(ax, wv, v, S, DH, D, DH);
  k_attn<<<S * H / 64, 64, 0, stream>>>(q, k, v, o);
  k_gemm<<<dim3(3, S), 192, 0, stream>>>(o, wo, out, S, D, D, 2 * D);
  k_rowdft<<<dim3(5, S), 64, 0, stream>>>(bx, X1);
  k_coldft<<<dim3(5, S), 64, 0, stream>>>(X1, X2, -1, wr, wi);
  k_coldft<<<dim3(5, S), 64, 0, stream>>>(X2, X3, 1, nullptr, nullptr);
  k_rowidft<<<dim3(9, S), 64, 0, stream>>>(X3, out);
}
```

```cpp
#include <hip/hip_runtime.h>
#include <hip/hip_bf16.h>
#include <stdint.h>
#include <utility>

#if defined(__HIP_DEVICE_COMPILE__)
namespace tab {
constexpr double PI = 3.14159265358979323846264338327950288;
constexpr double ccos(double x) {
  double x2 = x * x, term = 1.0, sum = 1.0;
  for (int i = 1; i <= 16; ++i) { term *= -x2 / ((2 * i - 1) * (2 * i)); sum += term; }
  return sum;
}
constexpr unsigned short tobf(double v) {
  float f = (float)v; unsigned u = __builtin_bit_cast(unsigned, f);
  return (unsigned short)((u + 0x7fffu + ((u >> 16) & 1u)) >> 16);
}
struct C576 { unsigned short c[1152], c2[1152]; };
constexpr C576 mk576() {
  C576 t{};
  for (int j = 0; j < 576; ++j) {
    int jj = j <= 288 ? j : j - 576; double v = ccos(2 * PI * jj / 576); if (j == 144 || j == 432) v = 0;
    t.c[j] = t.c[j + 576] = tobf(v); t.c2[j] = t.c2[j + 576] = tobf(2 * v);
  }
  return t;
}
constexpr C576 COS = mk576();
struct Frag { unsigned short v[512]; };
constexpr Frag fwd_frag(int idx) {
  Frag r{}; const int j = idx % 6, par = (idx / 6) & 1, s = (idx / 12) % 9, ch = idx / 108, part = j & 1;
  for (int l = 0; l < 64; ++l) {
    int f = 16 * (3 * ch + (j >> 1)) + (l & 15), off = part ? 144 : 0;
    if (f == 0 && part) { f = 144; off = par ? 144 : 0; }
    const int d0 = 2 * (32 * s + 8 * (l >> 4)) + par;
    int a = (f * d0) % 576; const int st = (2 * f) % 576;
    for (int e = 0; e < 8; ++e) { r.v[l * 8 + e] = COS.c[a + off]; a += st; if (a >= 576) a -= 576; }
  }
  return r;
}
constexpr Frag inv_frag(int idx) {
  Frag r{}; const int mt = idx % 6, par = (idx / 6) & 1, t = (idx / 12) % 9, ch = idx / 108;
  for (int l = 0; l < 64; ++l) {
    const int m = 16 * (6 * ch + mt) + (l & 15);
    int a = ((32 * t + 8 * (l >> 4) + par) * m) % 576; const int st = (2 * m) % 576;
    for (int e = 0; e < 4; ++e) { r.v[l * 8 + e] = COS.c2[a]; r.v[l * 8 + 4 + e] = COS.c2[a + 144]; a += st; if (a >= 576) a -= 576; }
    if (t == 0 && par == 0 && (l >> 4) == 0) { r.v[l * 8] = COS.c[0]; r.v[l * 8 + 4] = (m & 1) ? COS.c[288] : COS.c[0]; }
  }
  return r;
}
template <int R> constexpr Frag fwd_v = fwd_frag(R);
template <int R> constexpr Frag inv_v = inv_frag(R);
struct Tab { Frag f[324]; };
template <int... I> constexpr Tab mkfwd(std::integer_sequence<int, I...>) { return Tab{{fwd_v<I>...}}; }
template <int... I> constexpr Tab mkinv(std::integer_sequence<int, I...>) { return Tab{{inv_v<I>...}}; }
struct F64 { unsigned short v[16 * 64 * 8]; };
constexpr F64 mkf64() {
  F64 t{};
  for (int a = 0; a < 4; ++a) for (int s = 0; s < 4; ++s) for (int l = 0; l < 64; ++l) for (int e = 0; e < 4; ++e) {
    const int idx = (((16 * a + (l & 15)) * (16 * s + 4 * (l >> 4) + e)) & 63) * 9;
    t.v[((a * 4 + s) * 64 + l) * 8 + e] = COS.c[idx]; t.v[((a * 4 + s) * 64 + l) * 8 + 4 + e] = COS.c[idx + 432];
  }
  return t;
}
}
namespace {
__device__ const __attribute__((aligned(1024))) tab::Tab TAB_FWD = tab::mkfwd(std::make_integer_sequence<int, 324>{});
__device__ const __attribute__((aligned(1024))) tab::Tab TAB_INV = tab::mkinv(std::make_integer_sequence<int, 324>{});
__device__ const __attribute__((aligned(1024))) tab::F64 TAB_F64 = tab::mkf64();
}
#define P_FWD ((const char*)&TAB_FWD)
#define P_INV ((const char*)&TAB_INV)
#define P_F64 ((const char*)&TAB_F64)
#else
#define P_FWD ((const char*)0)
#define P_INV ((const char*)0)
#define P_F64 ((const char*)0)
#endif

namespace {
constexpr int S = 2048, D = 576, DH = 18, DF = 289;
constexpr int ROWB = D * 2;
constexpr int FP = 320;
constexpr int FP1 = 320;
constexpr int WK2 = 32 * DF;
typedef unsigned short u16;
typedef float f32x16 __attribute__((ext_vector_type(16)));
typedef float f32x4 __attribute__((ext_vector_type(4)));
typedef float f32x2 __attribute__((ext_vector_type(2)));
typedef unsigned u32x4 __attribute__((ext_vector_type(4)));
typedef unsigned u32x2 __attribute__((ext_vector_type(2)));
typedef __bf16 bf16x8 __attribute__((ext_vector_type(8)));
typedef __bf16 bf16x2 __attribute__((ext_vector_type(2)));
typedef short s16x4 __attribute__((ext_vector_type(4)));
#define LAS __attribute__((address_space(3)))
#define DI __device__ __forceinline__

DI int t1_column(int f) { const int g = f < 144 ? f : 288 - f; return f == 144 ? 288 : (g / 48) * 96 + (f < 144 ? 0 : 48) + g % 48; }
DI unsigned pk2(float lo, float hi) { f32x2 v = {lo, hi}; bf16x2 b = __builtin_convertvector(v, bf16x2); return __builtin_bit_cast(unsigned, b); }
DI float lo16(unsigned u) { return __uint_as_float(u << 16); }
DI float hi16(unsigned u) { return __uint_as_float(u & 0xffff0000u); }
DI f32x4 mfma16(u32x4 a, u32x4 b, f32x4 c) { return __builtin_amdgcn_mfma_f32_16x16x32_bf16(__builtin_bit_cast(bf16x8, a), __builtin_bit_cast(bf16x8, b), c, 0, 0, 0); }
DI u32x4 pk8(const f32x4& a, const f32x4& b) { u32x4 r = {pk2(a[0], a[1]), pk2(a[2], a[3]), pk2(b[0], b[1]), pk2(b[2], b[3])}; return r; }
DI void cs_rev(float rev, float& c, float& s) { c = __builtin_amdgcn_cosf(rev); s = __builtin_amdgcn_sinf(rev); }
template <bool WT> DI void st16(void* p, u32x4 v) {
  if constexpr (WT) asm volatile("global_store_dwordx4 %0, %1, off sc1\n\ts_nop 1" :: "v"(p), "v"(v) : "memory"); else *(u32x4*)p = v;
}
template <bool WT> DI void st8(void* p, u32x2 v) {
  if constexpr (WT) asm volatile("global_store_dwordx2 %0, %1, off sc1" :: "v"(p), "v"(v) : "memory"); else *(u32x2*)p = v;
}
#define WT_IMG 1
#define WT_Q 1
#define WT_OUT 1
#define WT_OUTF 1
#define T1_WT_N 6
#define WT_T2 1
#define SPEC 0
#define FFT_PRIO 2
#define WAIT_BARRIER() asm volatile("s_waitcnt lgkmcnt(0)\n\ts_barrier" ::: "memory")

constexpr int TILE_B = 2496;
constexpr int IMG_B = 163840;
constexpr float QSCALE = 0.23570226039551584f * 1.4426950408889634f;
constexpr float BOUND_MAX = 48.0f;

constexpr size_t WS_Q = 0, WS_IMG = 4u << 20, WS_KN = 5u << 20, WS_O = 6u << 20, WS_T1 = 10u << 20, WS_T2 = 14u << 20;

constexpr int LDS_A = 0, LDS_B = 9 * 8192, SLOT_B = 16384, NSLOT = 5, GEMM_LDS = LDS_B + NSLOT * SLOT_B;

struct Stager {
  const char* pa[2];
  const char* pb[4];
  unsigned sb[4];
  int ka, kb;
  unsigned lda, ldb;
};
DI void issue_a(Stager& g, int s) {
  unsigned keep;
  asm volatile(
    "s_mov_b32 %0, m0\n\t"
    "s_mov_b32 m0, %3\n\ts_nop 0\n\tglobal_load_lds_dwordx4 %1, off\n\t"
    "s_add_u32 m0, m0, 1024\n\ts_nop 0\n\tglobal_load_lds_dwordx4 %2, off\n\t"
    "s_mov_b32 m0, %0"
    : "=&s"(keep) : "v"(g.pa[0] + g.ka * 128), "v"(g.pa[1] + g.ka * 128), "s"(g.lda + s * 8192) : "scc", "memory");
  g.ka = g.ka == 8 ? 0 : g.ka + 1;
}
template <int PB>
DI void issue_b(Stager& g, int slot) {
  unsigned keep;
  if constexpr (PB == 3) {
    asm volatile(
      "s_mov_b32 %0, m0\n\t"
      "s_mov_b32 m0, %4\n\ts_nop 0\n\tglobal_load_lds_dwordx4 %1, off\n\t"
      "s_add_u32 m0, m0, 1024\n\ts_nop 0\n\tglobal_load_lds_dwordx4 %2, off\n\t"
      "s_add_u32 m0, m0, 1024\n\ts_nop 0\n\tglobal_load_lds_dwordx4 %3, off\n\t"
      "s_mov_b32 m0, %0"
      : "=&s"(keep) : "v"(g.pb[0] + (size_t)g.kb * g.sb[0]), "v"(g.pb[1] + (size_t)g.kb * g.sb[1]), "v"(g.pb[2] + (size_t)g.kb * g.sb[2]), "s"(g.ldb + slot * SLOT_B) : "scc", "memory");
  } else
  asm volatile(
    "s_mov_b32 %0, m0\n\t"
    "s_mov_b32 m0, %5\n\ts_nop 0\n\tglobal_load_lds_dwordx4 %1, off\n\t"
    "s_add_u32 m0, m0, 1024\n\ts_nop 0\n\tglobal_load_lds_dwordx4 %2, off\n\t"
    "s_add_u32 m0, m0, 1024\n\ts_nop 0\n\tglobal_load_lds_dwordx4 %3, off\n\t"
    "s_add_u32 m0, m0, 1024\n\ts_nop 0\n\tglobal_load_lds_dwordx4 %4, off\n\t"
    "s_mov_b32 m0, %0"
    : "=&s"(keep) : "v"(g.pb[0] + (size_t)g.kb * g.sb[0]), "v"(g.pb[1] + (size_t)g.kb * g.sb[1]), "v"(g.pb[2] + (size_t)g.kb * g.sb[2]), "v"(g.pb[3] + (size_t)g.kb * g.sb[3]),
      "s"(g.ldb + slot * SLOT_B) : "scc", "memory");
  g.kb = g.kb == 8 ? 0 : g.kb + 1;
}
DI int k_rotation(int rb, int c) { return (4 * rb + 2 * c) % 9; }
DI void stager_init(Stager& g, int p, int lane, const char* a, unsigned hi, unsigned lo, const char* b, bool table, bool kv, unsigned nchmask,
                    const char* b0, uintptr_t dk, uintptr_t dv, int rot) {
  g.ka = g.kb = rot;
#pragma unroll
  for (int i = 0; i < 2; ++i) {
    const unsigned row = (2 * p + i) * 8 + (lane >> 3), ch = (lane & 7) ^ (row & 7);
    g.pa[i] = a + ((row >> 5) * hi + (row & 31) * lo + ch * 16);
  }
  g.lda = LDS_A + 2 * p * 1024;
#pragma unroll
  for (int i = 0; i < 4; ++i) {
    const unsigned k = (4 * p + i) * 4 + (lane >> 4), u = (k & 3) | (((k >> 3) & 1) << 2), ch = ((lane & 15) ^ (u << 1)) & nchmask;
    const unsigned toff = (3 * p + i) * 1024 + lane * 16;
    const uintptr_t m = -(uintptr_t)(kv & (ch >= 8)), mk = -(uintptr_t)(ch < 12);
    const unsigned kvoff = k * (DH * 2) + ((ch & 2) ? 20 : (ch & 1) * 16), moff = k * ROWB + ch * 16;
    const uintptr_t wsrc = (((uintptr_t)b0 + ((dk & mk) | (dv & ~mk)) + kvoff) & m) | (((uintptr_t)b + moff) & ~m);
    g.pb[i] = table ? b + toff : (const char*)wsrc;
    g.sb[i] = table ? 12 * 1024 : (m ? 64 * DH * 2 : 64 * ROWB);
  }
  g.ldb = LDS_B + (table ? 3 : 4) * p * 1024;
}
template <int N> DI void vm_barrier() { asm volatile("s_waitcnt vmcnt(%0)\n\ts_barrier" :: "i"(N) : "memory"); }
template <int PB>
DI void gemm_producer(Stager& g
) {
  constexpr int P = 2 + PB;
  issue_a(g, 0); issue_b<PB>(g, 0); issue_a(g, 1); issue_b<PB>(g, 1);
  vm_barrier<P>();
  issue_a(g, 2); issue_b<PB>(g, 2);
  int slot = 3;
#pragma unroll 1
  for (int s = 3; s < 9; ++s) {
    issue_a(g, s); issue_b<PB>(g, slot);
    slot = slot == NSLOT - 1 ? 0 : slot + 1;
    vm_barrier<2 * P>();
  }
  vm_barrier<P>();
  asm volatile("s_waitcnt vmcnt(0)" ::: "memory");
  asm volatile("s_barrier" ::: "memory");
}
#define LABEL_ADDR(dst, name) asm volatile("s_getpc_b64 s[44:45]\n\ts_add_u32 s44, s44, " name "@rel32@lo+4\n\ts_addc_u32 s45, s45, " name "@rel32@hi+12\n\ts_mov_b64 %0, s[44:45]" : "=s"(dst) :: "s44", "s45", "scc")
DI void touch_code(uint64_t begin, uint64_t end, int y, int lane) {
  const int64_t off = (int64_t)(8 * y + (lane & 7)) * 64;
  asm volatile("global_load_dword v255, %0, off" :: "v"(begin + (off < (int64_t)(end - begin) ? off : 0)) : "v255", "memory");
}
DI void touch_word(const char* p) { asm volatile("global_load_dword v255, %0, off" :: "v"(p) : "v255", "memory"); }
#define BARRIER() asm volatile("s_barrier" ::: "memory")
template <int N, int K>
DI void sched_interleave() {
#pragma unroll
  for (int i = 0; i < N; ++i) { __builtin_amdgcn_sched_group_barrier(0x8, 1, 0); __builtin_amdgcn_sched_group_barrier(0x100, K, 0); }
}
#define SCHED_FENCE() __builtin_amdgcn_sched_barrier(0)

struct FragsW { u32x4 x[2][2]; u32x2 w[2][4][2]; };
DI void fragw_load(FragsW& f, LAS char* x0, LAS char* x1, LAS char* B, const int (&wa)[4]) {
#pragma unroll
  for (int ks = 0; ks < 2; ++ks) {
    LAS char* A = ks ? x1 : x0;
#pragma unroll
    for (int nt = 0; nt < 4; ++nt) {
      f.w[ks][nt][0] = __builtin_bit_cast(u32x2, __builtin_amdgcn_ds_read_tr16_b64_v4i16((LAS s16x4*)(B + wa[nt] + ks * 32 * 256)));
      f.w[ks][nt][1] = __builtin_bit_cast(u32x2, __builtin_amdgcn_ds_read_tr16_b64_v4i16((LAS s16x4*)(B + wa[nt] + ks * 32 * 256 + 4 * 256)));
      if (nt == 0) { f.x[ks][0] = *(LAS u32x4*)A; f.x[ks][1] = *(LAS u32x4*)(A + 2048); }
    }
  }
}
template <unsigned SWAPMASK>
DI void fragw_mma(const FragsW& f, f32x4 (&acc)[4][2]) {
#pragma unroll
  for (int ks = 0; ks < 2; ++ks)
#pragma unroll
    for (int nt = 0; nt < 4; ++nt) {
      const u32x4 wf = {f.w[ks][nt][0][0], f.w[ks][nt][0][1], f.w[ks][nt][1][0], f.w[ks][nt][1][1]};
#pragma unroll
      for (int mt = 0; mt < 2; ++mt)
        acc[nt][mt] = ((SWAPMASK >> nt) & 1) ? mfma16(f.x[ks][mt], wf, acc[nt][mt]) : mfma16(wf, f.x[ks][mt], acc[nt][mt]);
    }
}
DI int next_slot(int slot) { return slot == NSLOT - 1 ? 0 : slot + 1; }
template <unsigned SWAPMASK>
DI void gemm_consumer_w(LAS char* lds, int lane, int row0, int nt0, f32x4 (&acc)[4][2]
) {
  const int r = lane & 15, kq = lane >> 4, q = r >> 2, p = lane & 3;
#pragma unroll
  for (int nt = 0; nt < 4; ++nt) { acc[nt][0] = f32x4{0, 0, 0, 0}; acc[nt][1] = f32x4{0, 0, 0, 0}; }
  LAS char* x0 = lds + LDS_A + (row0 + r) * 128 + ((kq ^ (r & 7)) << 4);
  LAS char* x1 = lds + LDS_A + (row0 + r) * 128 + (((4 + kq) ^ (r & 7)) << 4);
  int wa[4];
#pragma unroll
  for (int nt = 0; nt < 4; ++nt) wa[nt] = (8 * kq + q) * 256 + (((nt0 + nt) ^ (q | ((kq & 1) << 2))) << 5) + 8 * p;
  FragsW f0, f1;
  BARRIER();
  fragw_load(f0, x0, x1, lds + LDS_B, wa);
  int slot = 0;
#pragma unroll 1
  for (int s = 0; s < 8; s += 2) {
    BARRIER();
    slot = next_slot(slot);
    fragw_load(f1, x0 + (s + 1) * 8192, x1 + (s + 1) * 8192, lds + LDS_B + slot * SLOT_B, wa);
    fragw_mma<SWAPMASK>(f0, acc);
    sched_interleave<10, 2>();
    SCHED_FENCE();
    BARRIER();
    slot = next_slot(slot);
    fragw_load(f0, x0 + (s + 2) * 8192, x1 + (s + 2) * 8192, lds + LDS_B + slot * SLOT_B, wa);
    fragw_mma<SWAPMASK>(f1, acc);
    sched_interleave<10, 2>();
    SCHED_FENCE();
  }
  fragw_mma<SWAPMASK>(f0, acc);
}
template <bool WT>
DI void store_tile(LAS char* lds, int lane, int w, const f32x4 (&acc)[4][2], float scale, char* dst, int pitch) {
  const int r = lane & 15, kq = lane >> 4;
  LAS char* st = lds + w * 4096;
#pragma unroll
  for (int mt = 0; mt < 2; ++mt)
#pragma unroll
    for (int nt = 0; nt < 4; ++nt)
      *(LAS u32x2*)(st + (16 * mt + r) * 128 + (((2 * nt + (kq >> 1)) ^ (r & 7)) << 4) + (kq & 1) * 8) =
          u32x2{pk2(acc[nt][mt][0] * scale, acc[nt][mt][1] * scale), pk2(acc[nt][mt][2] * scale, acc[nt][mt][3] * scale)};
  const int row = lane >> 3, ch = lane & 7;
  u32x4 v[4];
#pragma unroll
  for (int i = 0; i < 4; ++i) v[i] = *(LAS u32x4*)(st + (8 * i + row) * 128 + ((ch ^ row) << 4));
#pragma unroll
  for (int i = 0; i < 4; ++i) st16<WT>(dst + (8 * i + row) * pitch + ch * 16, v[i]);
}

__global__ void __launch_bounds__(512, 2) k_pre(const char* __restrict__ ax, const char* __restrict__ wq, const char* __restrict__ wk, const char* __restrict__ wv,
                                               const char* __restrict__ bx, char* __restrict__ ws) {
  extern __shared__ __attribute__((aligned(16))) char smem[];
  LAS char* lds = (LAS char*)smem;
  const int lane = threadIdx.x & 63, w = __builtin_amdgcn_readfirstlane(threadIdx.x >> 6);
  asm volatile("lbl_pre_begin:");
  if (w < 2) {
    uint64_t b0, b1, b2; LABEL_ADDR(b0, "lbl_pre_begin"); LABEL_ADDR(b1, "lbl_mid_begin"); LABEL_ADDR(b2, "lbl_post_begin");
    touch_code(w ? b1 : b0, w ? b2 : b1, blockIdx.x >> 3, lane);
  }
  if (w == 2) touch_code((uint64_t)P_F64, (uint64_t)P_F64 + 16384, blockIdx.x >> 3, lane);
  asm volatile("" :: "s"(ax), "s"(wq), "s"(wk), "s"(wv), "s"(bx), "s"(ws));
  const int xcd = blockIdx.x & 7, y = blockIdx.x >> 3;
  const bool proj = SPEC ? xcd < 5 : y < 20;
  const int q = SPEC ? (proj ? xcd : xcd - 5) * 32 + y : (proj ? xcd * 20 + y : xcd * 12 + y - 20), rb = proj ? q / 5 : q / 3, c = proj ? q % 5 : q % 3;
  if (__builtin_expect(w >= 4, 1)) {
    Stager g;
    stager_init(g, w - 4, lane, proj ? ax + rb * 64 * ROWB : bx + 2 * rb * ROWB, proj ? 32 * ROWB : ROWB, proj ? ROWB : 64 * ROWB,
                proj ? wq + c * 256 : P_FWD + c * 108 * 1024, !proj, proj && c == 4, 15, wq, (uintptr_t)wk - (uintptr_t)wq, (uintptr_t)wv - (uintptr_t)wq,
                k_rotation(rb, c));
    if (proj) gemm_producer<4>(g
      );
    else gemm_producer<3>(g
      );
    return;
  }
  const int r = lane & 15, kq = lane >> 4;
  if (proj) {
    const int wm = w & 1, wn = w >> 1;
    f32x4 acc[4][2];
    char* qo = ws + WS_Q + (size_t)(rb * 64 + 32 * wm) * ROWB + c * 256 + wn * 128;
    if (c == 4 && wn == 1) {
      gemm_consumer_w<0xc>(lds, lane, 32 * wm, 4, acc
        );
      unsigned n2max = 0;
#pragma unroll
      for (int mt = 0; mt < 2; ++mt) {
        const int tok = rb * 64 + 32 * wm + 16 * mt;
        const int key = tok + r;
        char* tile = ws + WS_IMG + (key >> 5) * TILE_B;
        const u32x2 km = {pk2(acc[0][mt][0], acc[0][mt][1]), pk2(acc[0][mt][2], acc[0][mt][3])};
        st8<WT_IMG>(tile + ((kq >> 1) * 32 + (key & 31)) * 16 + (kq & 1) * 8, km);
        const unsigned kr = pk2(acc[1][mt][2], acc[1][mt][3]);
        if (kq == 1) st8<WT_IMG>(tile + 1024 + (key & 31) * 8, u32x2{kr, 0x00003f80u});
        float n2 = lo16(km[0]) * lo16(km[0]) + hi16(km[0]) * hi16(km[0]) + lo16(km[1]) * lo16(km[1]) + hi16(km[1]) * hi16(km[1]);
        if (kq == 1) n2 += lo16(kr) * lo16(kr) + hi16(kr) * hi16(kr);
        { const auto t = __builtin_amdgcn_permlane16_swap(__float_as_uint(n2), __float_as_uint(n2), false, false); n2 = __uint_as_float(t[0]) + __uint_as_float(t[1]); }
        { const auto t = __builtin_amdgcn_permlane32_swap(__float_as_uint(n2), __float_as_uint(n2), false, false); n2 = __uint_as_float(t[0]) + __uint_as_float(t[1]); }
        n2max = max(n2max, __float_as_uint(n2));
        char* vt = ws + WS_IMG + (tok >> 5) * TILE_B + 1280 + ((tok >> 4) & 1) * 608 + (kq & 1) * 304 + (kq >> 1) * 8;
        const int vrow = (r & 3) | ((r & 8) >> 1) | ((r & 4) << 1);
        st8<WT_IMG>(vt + vrow * 16, u32x2{pk2(acc[2][mt][0], acc[2][mt][1]), pk2(acc[2][mt][2], acc[2][mt][3])});
        if (r == 6 || r == 7) st8<WT_IMG>(vt + (10 + r) * 16, u32x2{pk2(acc[3][mt][0], acc[3][mt][1]), pk2(acc[3][mt][2], acc[3][mt][3])});
        if (r == 8) st8<WT_IMG>(vt + 18 * 16, u32x2{0x3f803f80u, 0x3f803f80u});
      }
      n2max = max(n2max, (unsigned)__builtin_amdgcn_update_dpp(0, (int)n2max, 0x111, 0xf, 0xf, true));
      n2max = max(n2max, (unsigned)__builtin_amdgcn_update_dpp(0, (int)n2max, 0x112, 0xf, 0xf, true));
      n2max = max(n2max, (unsigned)__builtin_amdgcn_update_dpp(0, (int)n2max, 0x114, 0xf, 0xf, true));
      n2max = max(n2max, (unsigned)__builtin_amdgcn_update_dpp(0, (int)n2max, 0x118, 0xf, 0xf, true));
      if (lane == 15) ((unsigned*)(ws + WS_KN))[rb * 2 + wm] = n2max;
    } else {
      gemm_consumer_w<0>(lds, lane, 32 * wm, 4 * wn, acc
        );
      store_tile<WT_Q>(lds, lane, w, acc, QSCALE, qo, ROWB);
    }
  } else {
    const int gq = w & 1, par = w >> 1, n2 = 2 * rb + gq;
    u32x4 Gr[2], Gi[2];
#pragma unroll
    for (int kt = 0; kt < 2; ++kt) {
      float cr[8], ci[8];
      const float k1f = (float)(16 * kt + r), n0f = (float)(n2 + 256 * kq) * (1.0f / 2048.0f);
#pragma unroll
      for (int j = 0; j < 8; ++j) { float cc, ss; cs_rev(k1f * (n0f + (float)(16 * (j >> 2) + (j & 3)) * (1.0f / 32.0f)), cc, ss); cr[j] = cc; ci[j] = -ss; }
#pragma unroll
      for (int j = 0; j < 4; ++j) { Gr[kt][j] = pk2(cr[2 * j], cr[2 * j + 1]); Gi[kt][j] = pk2(ci[2 * j], ci[2 * j + 1]); }
    }
    asm volatile("" : "+v"(Gr[0]), "+v"(Gr[1]), "+v"(Gi[0]), "+v"(Gi[1]));
    f32x4 accE[3][2], accO[3][2];
#pragma unroll
    for (int i = 0; i < 3; ++i)
#pragma unroll
      for (int mt = 0; mt < 2; ++mt) { accE[i][mt] = f32x4{0, 0, 0, 0}; accO[i][mt] = f32x4{0, 0, 0, 0}; }
    LAS char* x0 = lds + LDS_A + (32 * gq + r) * 128 + (((2 * kq) ^ (r & 7)) << 4);
    LAS char* x1 = lds + LDS_A + (32 * gq + r) * 128 + (((2 * kq + 1) ^ (r & 7)) << 4);
    LAS char* tb = lds + LDS_B + par * 1024 + lane * 16;
    struct FF { u32x4 lo[2], hi[2], te[3], to[3]; } f0, f1;
    auto load = [&](FF& d, int s, int slot) {
#pragma unroll
      for (int mt = 0; mt < 2; ++mt) { d.lo[mt] = *(LAS u32x4*)(x0 + s * 8192 + mt * 2048); d.hi[mt] = *(LAS u32x4*)(x1 + s * 8192 + mt * 2048); }
#pragma unroll
      for (int i = 0; i < 3; ++i) { d.te[i] = *(LAS u32x4*)(tb + slot * SLOT_B + i * 2048); d.to[i] = *(LAS u32x4*)(tb + slot * SLOT_B + 6144 + i * 2048); }
    };
    auto mma = [&](const FF& d) {
#pragma unroll
      for (int mt = 0; mt < 2; ++mt) {
        const u32x4 xe = {__builtin_amdgcn_perm(d.lo[mt][1], d.lo[mt][0], 0x05040100u), __builtin_amdgcn_perm(d.lo[mt][3], d.lo[mt][2], 0x05040100u),
                          __builtin_amdgcn_perm(d.hi[mt][1], d.hi[mt][0], 0x05040100u), __builtin_amdgcn_perm(d.hi[mt][3], d.hi[mt][2], 0x05040100u)};
        const u32x4 xo = {__builtin_amdgcn_perm(d.lo[mt][1], d.lo[mt][0], 0x07060302u), __builtin_amdgcn_perm(d.lo[mt][3], d.lo[mt][2], 0x07060302u),
                          __builtin_amdgcn_perm(d.hi[mt][1], d.hi[mt][0], 0x07060302u), __builtin_amdgcn_perm(d.hi[mt][3], d.hi[mt][2], 0x07060302u)};
#pragma unroll
        for (int i = 0; i < 3; ++i) { accE[i][mt] = mfma16(xe, d.te[i], accE[i][mt]); accO[i][mt] = mfma16(xo, d.to[i], accO[i][mt]); }
      }
    };
    BARRIER();
    load(f0, 0, 0);
    int slot = 0;
#pragma unroll 1
    for (int s = 0; s < 8; s += 2) {
      BARRIER();
      slot = next_slot(slot);
      load(f1, s + 1, slot);
      mma(f0);
      sched_interleave<10, 1>();
      SCHED_FENCE();
      BARRIER();
      slot = next_slot(slot);
      load(f0, s + 2, slot);
      mma(f1);
      sched_interleave<10, 1>();
      SCHED_FENCE();
    }
    mma(f0);
    const float sg = par ? -1.0f : 1.0f;
    u32x4 fa[3], fb[3];
#pragma unroll
    for (int i = 0; i < 3; ++i) {
      f32x4 a[2], b[2];
#pragma unroll
      for (int mt = 0; mt < 2; ++mt) {
        a[mt] = accE[i][mt] + accO[i][mt]; b[mt] = (accE[i][mt] - accO[i][mt]) * sg;
        if (i == 0 && par == 1 && c == 0 && r == 0) { a[mt] = f32x4{0, 0, 0, 0}; b[mt] = f32x4{0, 0, 0, 0}; }
      }
      fa[i] = pk8(a[0], a[1]); fb[i] = pk8(b[0], b[1]);
    }
#pragma unroll
    for (int i = 0; i < 3; ++i) *(LAS u32x4*)(lds + (w * 3 + i) * 1024 + lane * 16) = par ? fa[i] : fb[i];
    WAIT_BARRIER();
    auto dft32 = [&](const u32x4& bre, const u32x4& bim, u32x4 (&z)[2]) {
      const u32x4 bin = {bim[0] ^ 0x80008000u, bim[1] ^ 0x80008000u, bim[2] ^ 0x80008000u, bim[3] ^ 0x80008000u};
#pragma unroll
      for (int kt = 0; kt < 2; ++kt) {
        f32x4 zr = {0, 0, 0, 0}, zi = {0, 0, 0, 0};
        zr = mfma16(bre, Gr[kt], zr); zi = mfma16(bre, Gi[kt], zi);
        zr = mfma16(bin, Gi[kt], zr); zi = mfma16(bim, Gr[kt], zi);
        z[kt] = u32x4{pk2(zr[0], zi[0]), pk2(zr[1], zi[1]), pk2(zr[2], zi[2]), pk2(zr[3], zi[3])};
      }
    };
    LAS char* st = lds + 49152 + gq * 12288;
    char* t1 = ws + WS_T1 + (size_t)n2 * 32 * FP1 * 4;
    u32x4 other[3], z[3][2];
#pragma unroll
    for (int i = 0; i < 3; ++i) other[i] = *(LAS u32x4*)(lds + ((w ^ 2) * 3 + i) * 1024 + lane * 16);
#pragma unroll
    for (int i = 0; i < 3; ++i) dft32(par ? other[i] : fa[i], par ? fb[i] : other[i], z[i]);
#pragma unroll
    for (int i = 0; i < 3; ++i)
#pragma unroll
      for (int kt = 0; kt < 2; ++kt) *(LAS u32x4*)(st + (16 * kt + r) * 384 + (((par * 12 + 4 * i + kq) ^ (r & 7)) << 4)) = z[i][kt];
    if (c == 0 && par == 1) {
      u32x4 zx[2];
      dft32(pk8(accE[0][0], accE[0][1]), pk8(accO[0][0], accO[0][1]), zx);
      if (kq == 0) {
#pragma unroll
        for (int kt = 0; kt < 2; ++kt) *(unsigned*)(t1 + ((16 * kt + r) * FP1 + 288) * 4) = zx[kt][0];
      }
    }
    WAIT_BARRIER();
    const int row8 = lane >> 3, ch8 = lane & 7;
    u32x4 v[6];
#pragma unroll
    for (int j = 0; j < 6; ++j) v[j] = *(LAS u32x4*)(st + (16 * par + 8 * (j & 1) + row8) * 384 + ((8 * (j >> 1) + (ch8 ^ row8)) << 4));
#pragma unroll
    for (int j = 0; j < 6; ++j) {
      char* dst = t1 + (16 * par + 8 * (j & 1) + row8) * (FP1 * 4) + c * 384 + (8 * (j >> 1) + ch8) * 16;
      if (j < T1_WT_N) st16<true>(dst, v[j]); else st16<false>(dst, v[j]);
    }
  }
}

DI f32x16 mfma32(u32x4 a, u32x4 b, f32x16 c) { return __builtin_amdgcn_mfma_f32_32x32x16_bf16(__builtin_bit_cast(bf16x8, a), __builtin_bit_cast(bf16x8, b), c, 0, 0, 0); }
DI f32x16 mfma32x8(u32x2 a, u32x2 b, f32x16 c) { return __builtin_amdgcn_mfma_f32_32x32x8bf16_1k(__builtin_bit_cast(s16x4, a), __builtin_bit_cast(s16x4, b), c, 0, 0, 0); }

DI void fft_mid(char* __restrict__ ws, const u16* __restrict__ wr, const u16* __restrict__ wi, int task, int lane) {
  const int r = lane & 15, kq = lane >> 4;
  const int k1 = task < 576 ? task / 18 : 16 * (task - 576) + r, f = task < 576 ? 16 * (task % 18) + r : 288;
  constexpr int TROW1 = 32 * FP1 * 4, TROW = 32 * FP * 4, WROW = WK2 * 2;
  const __amdgpu_buffer_rsrc_t rt1 = __builtin_amdgcn_make_buffer_rsrc(ws + WS_T1, 0, 64 * TROW1, 0x00020000);
  const __amdgpu_buffer_rsrc_t rt2 = __builtin_amdgcn_make_buffer_rsrc(ws + WS_T2, 0, 64 * TROW, 0x00020000);
  const __amdgpu_buffer_rsrc_t rwr = __builtin_amdgcn_make_buffer_rsrc((void*)wr, 0, S * DF * 2, 0x00020000);
  const __amdgpu_buffer_rsrc_t rwi = __builtin_amdgcn_make_buffer_rsrc((void*)wi, 0, S * DF * 2, 0x00020000);
  const __amdgpu_buffer_rsrc_t rf = __builtin_amdgcn_make_buffer_rsrc((void*)P_F64, 0, 16384, 0x00020000);
  const int toff = 4 * kq * TROW1 + (k1 * FP1 + t1_column(f)) * 4, woff = 4 * kq * WROW + (k1 * DF + f) * 2;
  unsigned d[4][4];
#pragma unroll
  for (int s = 0; s < 4; ++s)
#pragma unroll
    for (int i = 0; i < 4; ++i) d[s][i] = __builtin_amdgcn_raw_buffer_load_b32(rt1, toff, (16 * s + i) * TROW1, 0);
  u32x4 cs[4][4];
#pragma unroll
  for (int a = 0; a < 4; ++a)
#pragma unroll
    for (int s = 0; s < 4; ++s) cs[a][s] = __builtin_amdgcn_raw_buffer_load_b128(rf, lane * 16, (a * 4 + s) * 1024, 0);
  u16 wre[4][4], wim[4][4];
#pragma unroll
  for (int a = 0; a < 4; ++a)
#pragma unroll
    for (int e = 0; e < 4; ++e) {
      wre[a][e] = __builtin_amdgcn_raw_buffer_load_b16(rwr, woff, (16 * a + e) * WROW, 0);
      wim[a][e] = __builtin_amdgcn_raw_buffer_load_b16(rwi, woff, (16 * a + e) * WROW, 0);
    }
  u32x4 b1[4], b2[4];
#pragma unroll
  for (int s = 0; s < 4; ++s) {
    const unsigned re01 = __builtin_amdgcn_perm(d[s][1], d[s][0], 0x05040100u), re23 = __builtin_amdgcn_perm(d[s][3], d[s][2], 0x05040100u);
    const unsigned im01 = __builtin_amdgcn_perm(d[s][1], d[s][0], 0x07060302u), im23 = __builtin_amdgcn_perm(d[s][3], d[s][2], 0x07060302u);
    b1[s] = u32x4{re01, re23, im01, im23};
    b2[s] = u32x4{im01, im23, re01 ^ 0x80008000u, re23 ^ 0x80008000u};
  }
  u32x4 y1[4], y2[4];
#pragma unroll
  for (int a = 0; a < 4; ++a) {
    f32x4 xr = {0, 0, 0, 0}, xi = {0, 0, 0, 0};
#pragma unroll
    for (int s = 0; s < 4; ++s) { xr = mfma16(cs[a][s], b1[s], xr); xi = mfma16(cs[a][s], b2[s], xi); }
    f32x4 yr, yi;
#pragma unroll
    for (int e = 0; e < 4; ++e) {
      const float p = __uint_as_float((unsigned)wre[a][e] << 16), q = __uint_as_float((unsigned)wim[a][e] << 16);
      yr[e] = xr[e] * p - xi[e] * q; yi[e] = xr[e] * q + xi[e] * p;
    }
    y1[a] = u32x4{pk2(yr[0], yr[1]), pk2(yr[2], yr[3]), pk2(-yi[0], -yi[1]), pk2(-yi[2], -yi[3])};
    y2[a] = u32x4{pk2(yi[0], yi[1]), pk2(yi[2], yi[3]), pk2(yr[0], yr[1]), pk2(yr[2], yr[3])};
  }
#pragma unroll
  for (int a = 0; a < 4; ++a) {
    f32x4 ur = {0, 0, 0, 0}, ui = {0, 0, 0, 0};
#pragma unroll
    for (int s = 0; s < 4; ++s) { ur = mfma16(y1[s], cs[a][s], ur); ui = mfma16(y2[s], cs[a][s], ui); }
    const u32x4 v = {pk2(ur[0], ui[0]), pk2(ur[1], ui[1]), pk2(ur[2], ui[2]), pk2(ur[3], ui[3])};
    if (task < 576) __builtin_amdgcn_raw_buffer_store_b128(v, rt2, r * TROW + (k1 * FP + f - r + 4 * kq) * 4, 16 * a * TROW, WT_T2 ? 16 : 0);
    else {
#pragma unroll
      for (int e = 0; e < 4; ++e) __builtin_amdgcn_raw_buffer_store_b32(v[e], rt2, r * TROW + ((16 * (task - 576) + 4 * kq + e) * FP + 288) * 4, 16 * a * TROW, WT_T2 ? 16 : 0);
    }
  }
}

DI void attn_slow(LAS char* lds, u32x4 qm, unsigned qr, int lane, float (&o)[11]) {
  const int r = lane & 31, h = lane >> 5;
  const u32x2 qrem = {h == 0 ? qr : 0u, 0u};
  f32x16 acc;
#pragma unroll
  for (int i = 0; i < 16; ++i) acc[i] = 0.f;
  float m = -INFINITY;
#pragma unroll 1
  for (int t = 0; t < 64; ++t) {
    LAS char* tb = lds + t * TILE_B;
    f32x16 s;
#pragma unroll
    for (int i = 0; i < 16; ++i) s[i] = 0.f;
    s = mfma32(*(LAS u32x4*)(tb + lane * 16), qm, s);
    s = mfma32x8(*(LAS u32x2*)(tb + 1024 + r * 8), qrem, s);
    float mx = s[0];
#pragma unroll
    for (int i = 1; i < 16; ++i) mx = (s[i] > mx || s[i] != s[i]) ? s[i] : mx;
    { const float t2 = __shfl_xor(mx, 32); mx = (t2 > mx || t2 != t2) ? t2 : mx; }
    const float mn = (mx > m || mx != mx) ? mx : m;
    const float shift = mn == -INFINITY ? 0.f : mn;
    const float alpha = __builtin_amdgcn_exp2f(m - shift);
    m = mn;
    u32x4 p0, p1;
#pragma unroll
    for (int j = 0; j < 4; ++j) {
      p0[j] = pk2(__builtin_amdgcn_exp2f(s[2 * j] - shift), __builtin_amdgcn_exp2f(s[2 * j + 1] - shift));
      p1[j] = pk2(__builtin_amdgcn_exp2f(s[8 + 2 * j] - shift), __builtin_amdgcn_exp2f(s[9 + 2 * j] - shift));
    }
#pragma unroll
    for (int i = 0; i < 16; ++i) acc[i] *= alpha;
    LAS char* vb = tb + 1280 + h * 304 + min(r, 18) * 16;
    acc = mfma32(*(LAS u32x4*)vb, p0, acc);
    acc = mfma32(*(LAS u32x4*)(vb + 608), p1, acc);
  }
#pragma unroll
  for (int i = 0; i < 11; ++i) o[i] = acc[i];
}

constexpr int LDS_FLAG = 159744;
__global__ void __launch_bounds__(512, 2) k_mid(char* __restrict__ ws, const u16* __restrict__ wr, const u16* __restrict__ wi, const char* __restrict__ wo) {
  extern __shared__ __attribute__((aligned(16))) char smem[];
  const int tid = threadIdx.x, lane = tid & 63;
  const int w = __builtin_amdgcn_readfirstlane(tid >> 6);
  asm volatile("lbl_mid_begin:");
  asm volatile("" :: "s"(ws), "s"(wr), "s"(wi), "s"(wo));
  if (w >= 4) {
    const char* gb = ws + WS_IMG + (w - 4) * 1024;
    unsigned voff = lane * 16, keep, ldsb = (w - 4) * 1024, fadr = LDS_FLAG + (w - 4) * 4, tmp;
#define IMG_PIECES(n, step) "s_mov_b32 s42, " #n "\n\t1:\n\tglobal_load_lds_dwordx4 %[voff], s[40:41]\n\ts_add_u32 m0, m0, " #step "\n\t" \
                      "s_add_u32 s40, s40, " #step "\n\ts_addc_u32 s41, s41, 0\n\ts_sub_u32 s42, s42, 1\n\ts_cmp_lg_u32 s42, 0\n\ts_cbranch_scc1 1b\n\t"
#define IMG_FLAG(v) "v_mov_b32 %[tmp], " #v "\n\tds_write_b32 %[fadr], %[tmp]\n\t"
    asm volatile(
      "s_mov_b32 %[keep], m0\n\t"
      "s_mov_b32 s40, %[glo]\n\ts_mov_b32 s41, %[ghi]\n\t"
      "s_mov_b32 m0, %[ldsb]\n\t"
      IMG_FLAG(0)
      IMG_PIECES(7, 4096) "s_waitcnt lgkmcnt(0)\n\ts_barrier\n\ts_waitcnt vmcnt(3)\n\t" IMG_FLAG(1)
      "s_waitcnt vmcnt(0)\n\t"
      "s_cmp_eq_u32 %[w], 7\n\ts_cbranch_scc1 2f\n\t"
      IMG_FLAG(15)
      "s_branch 3f\n\t"
      "2:\n\t"
      IMG_FLAG(2)
      "s_sub_u32 m0, m0, 3072\n\ts_sub_u32 s40, s40, 3072\n\ts_subb_u32 s41, s41, 0\n\t"
      IMG_PIECES(25, 1024) "s_waitcnt vmcnt(8)\n\t" IMG_FLAG(3)
      IMG_PIECES(20, 1024) "s_waitcnt vmcnt(8)\n\t" IMG_FLAG(4)
      IMG_PIECES(19, 1024) "s_waitcnt vmcnt(8)\n\t" IMG_FLAG(5)
      IMG_PIECES(20, 1024) "s_waitcnt vmcnt(8)\n\t" IMG_FLAG(6)
      IMG_PIECES(19, 1024) "s_waitcnt vmcnt(8)\n\t" IMG_FLAG(7)
      IMG_PIECES(20, 1024) "s_waitcnt vmcnt(8)\n\t" IMG_FLAG(8)
      IMG_PIECES(5, 1024)  "s_waitcnt vmcnt(0)\n\t" IMG_FLAG(15)
      "3:\n\t"
      "s_waitcnt lgkmcnt(0)\n\t"
      "s_mov_b32 m0, %[keep]"
      : [keep] "=&s"(keep), [tmp] "=&v"(tmp)
      : [voff] "v"(voff), [fadr] "v"(fadr), [glo] "s"((unsigned)(uintptr_t)gb), [ghi] "s"((unsigned)((uintptr_t)gb >> 32)), [ldsb] "s"(ldsb), [w] "s"(w)
      : "s40", "s41", "s42", "scc", "memory");
    if (w == 7) {
      const int oo = (blockIdx.x * 64 + lane) * 64;
      const char* src = oo < D * ROWB ? wo + oo : P_INV + (oo - D * ROWB < 324 * 1024 ? oo - D * ROWB : 0);
      touch_word(src);
      uint64_t b, e, e2; LABEL_ADDR(b, "lbl_post_begin"); LABEL_ADDR(e, "lbl_post_end"); LABEL_ADDR(e2, "lbl_post_end2");
      touch_code(b, e > e2 ? e : e2, blockIdx.x >> 3, lane);
    }
    const int task = w == 7 ? 1024 : blockIdx.x + 256 * (w - 4);
    if (task < 578) {
      __builtin_amdgcn_s_setprio(FFT_PRIO);
      fft_mid(ws, wr, wi, task, lane);
    }
    return;
  }
  const int r = lane & 31, h = lane >> 5;
  const int tokA = blockIdx.x * 8 + 2 * w;
  const char* qrowA = ws + WS_Q + tokA * ROWB + r * 36;
  const u32x4 qa = *(const u32x4*)(qrowA + 16 * h);
  const u32x4 qb = *(const u32x4*)(qrowA + ROWB + 16 * h);
  const unsigned qr = *(const unsigned*)(qrowA + h * ROWB + 32);
  unsigned kb = ((const unsigned*)(ws + WS_KN))[lane];
  float pa = 0.f, pb = 0.f;
#pragma unroll
  for (int j = 0; j < 4; ++j) { float a = lo16(qa[j]), b = hi16(qa[j]); pa = fmaf(a, a, fmaf(b, b, pa)); a = lo16(qb[j]); b = hi16(qb[j]); pb = fmaf(a, a, fmaf(b, b, pb)); }
  const auto sw = __builtin_amdgcn_permlane32_swap(__float_as_uint(pa), __float_as_uint(pb), false, false);
  float qq = __uint_as_float(sw[0]) + __uint_as_float(sw[1]);
  { const float a = lo16(qr), b = hi16(qr); qq = fmaf(a, a, fmaf(b, b, qq)); }
  kb = max(kb, (unsigned)__builtin_amdgcn_update_dpp(0, (int)kb, 0x111, 0xf, 0xf, true));
  kb = max(kb, (unsigned)__builtin_amdgcn_update_dpp(0, (int)kb, 0x112, 0xf, 0xf, true));
  kb = max(kb, (unsigned)__builtin_amdgcn_update_dpp(0, (int)kb, 0x114, 0xf, 0xf, true));
  kb = max(kb, (unsigned)__builtin_amdgcn_update_dpp(0, (int)kb, 0x118, 0xf, 0xf, true));
  const unsigned kmaxb = max(max((unsigned)__builtin_amdgcn_readlane((int)kb, 15), (unsigned)__builtin_amdgcn_readlane((int)kb, 31)),
                             max((unsigned)__builtin_amdgcn_readlane((int)kb, 47), (unsigned)__builtin_amdgcn_readlane((int)kb, 63)));
  const float bnd = __builtin_amdgcn_sqrtf(qq * __uint_as_float(kmaxb)) * 1.01f + 1e-3f;
  float oa[11], ob[11];
  WAIT_BARRIER();
  if (__builtin_amdgcn_readfirstlane(__builtin_popcountll(__ballot(!(bnd <= BOUND_MAX)))) != 0) {
    for (;;) {
      const u32x4 st = *(volatile LAS u32x4*)((LAS char*)smem + LDS_FLAG);
      if (__builtin_amdgcn_readfirstlane(min(min(st[0], st[1]), min(st[2], st[3]))) >= 15) break;
      __builtin_amdgcn_s_sleep(2);
    }
    const unsigned qrB = __shfl_xor(qr, 32);
    attn_slow((LAS char*)smem, qa, qr, lane, oa);
    attn_slow((LAS char*)smem, qb, h == 0 ? qrB : qr, lane, ob);
  } else {
    const unsigned mb = ((__float_as_uint(bnd) + 0xffffu) >> 16) | 0x8000u;
    const unsigned aK = lane * 16, aR = 1024 + r * 8, aV = 1280 + h * 304 + min(r, 18) * 16;
    asm volatile(
      "v_mov_b32 v32, %[qa0]\n\tv_mov_b32 v33, %[qa1]\n\tv_mov_b32 v34, %[qa2]\n\tv_mov_b32 v35, %[qa3]\n\t"
      "v_mov_b32 v36, %[qb0]\n\tv_mov_b32 v37, %[qb1]\n\tv_mov_b32 v38, %[qb2]\n\tv_mov_b32 v39, %[qb3]\n\t"
      "v_mov_b32 v40, %[qr0]\n\tv_mov_b32 v41, %[qr1]\n\t"
      "v_mov_b32 v216, %[aK]\n\tv_mov_b32 v217, %[aR]\n\tv_mov_b32 v218, %[aV]\n\tv_mov_b32 v219, %[aF]\n\t"
    "s_setprio 1\n\t"
    "v_mov_b32 v120, 0\n\t"
    "v_mov_b32 v121, 0\n\t"
    "v_mov_b32 v122, 0\n\t"
    "v_mov_b32 v123, 0\n\t"
    "v_mov_b32 v124, 0\n\t"
    "v_mov_b32 v125, 0\n\t"
    "v_mov_b32 v126, 0\n\t"
    "v_mov_b32 v127, 0\n\t"
    "v_mov_b32 v128, 0\n\t"
    "v_mov_b32 v129, 0\n\t"
    "v_mov_b32 v130, 0\n\t"
    "v_mov_b32 v131, 0\n\t"
    "v_mov_b32 v132, 0\n\t"
    "v_mov_b32 v133, 0\n\t"
    "v_mov_b32 v134, 0\n\t"
    "v_mov_b32 v135, 0\n\t"
    "v_mov_b32 v100, 0\n\t"
    "v_mov_b32 v101, 0\n\t"
    "v_mov_b32 v102, 0\n\t"
    "v_mov_b32 v103, 0\n\t"
    "v_mov_b32 v76, 0\n\t"
    "v_mov_b32 v77, 0\n\t"
    "v_mov_b32 v78, 0\n\t"
    "v_mov_b32 v79, 0\n\t"
    "v_mov_b32 v136, 0\n\t"
    "v_mov_b32 v137, 0\n\t"
    "v_mov_b32 v138, 0\n\t"
    "v_mov_b32 v139, 0\n\t"
    "v_mov_b32 v140, 0\n\t"
    "v_mov_b32 v141, 0\n\t"
    "v_mov_b32 v142, 0\n\t"
    "v_mov_b32 v143, 0\n\t"
    "v_mov_b32 v144, 0\n\t"
    "v_mov_b32 v145, 0\n\t"
    "v_mov_b32 v146, 0\n\t"
    "v_mov_b32 v147, 0\n\t"
    "v_mov_b32 v148, 0\n\t"
    "v_mov_b32 v149, 0\n\t"
    "v_mov_b32 v150, 0\n\t"
    "v_mov_b32 v151, 0\n\t"
    "v_mov_b32 v116, 0\n\t"
    "v_mov_b32 v117, 0\n\t"
    "v_mov_b32 v118, 0\n\t"
    "v_mov_b32 v119, 0\n\t"
    "v_mov_b32 v84, 0\n\t"
    "v_mov_b32 v85, 0\n\t"
    "v_mov_b32 v86, 0\n\t"
    "v_mov_b32 v87, 0\n\t"
    "v_mov_b32 v68, 0\n\t"
    "v_mov_b32 v69, 0\n\t"
    "v_mov_b32 v70, 0\n\t"
    "v_mov_b32 v71, 0\n\t"
    "7:\n\t"
    "ds_read_b128 v[220:223], v219\n\t"
    "s_waitcnt lgkmcnt(0)\n\t"
    "v_min_u32 v220, v220, v221\n\t"
    "v_min3_u32 v220, v220, v222, v223\n\t"
    "s_nop 0\n\t"
    "v_readfirstlane_b32 s43, v220\n\t"
    "s_nop 3\n\t"
    "s_cmp_ge_u32 s43, 1\n\t"
    "s_cbranch_scc0 7b\n\t"
    "ds_read_b128 v[44:47], v216 offset:0\n\t"
    "ds_read_b64 v[42:43], v217 offset:0\n\t"
    "ds_read_b128 v[48:51], v216 offset:2496\n\t"
    "ds_read_b64 v[52:53], v217 offset:2496\n\t"
    "ds_read_b128 v[56:59], v218 offset:0\n\t"
    "s_waitcnt lgkmcnt(0)\n\t"
    "v_mfma_f32_32x32x16_bf16 v[152:167], v[44:47], v[32:35], 0\n\t"
    "v_mfma_f32_32x32x16_bf16 v[168:183], v[44:47], v[36:39], 0\n\t"
    "s_nop 7\n\t"
    "s_nop 3\n\t"
    "v_mfma_f32_32x32x4bf16_1k v[152:183], v[42:43], v[40:41], v[152:183]\n\t"
    "s_nop 7\n\t"
    "s_nop 7\n\t"
    "s_nop 7\n\t"
    "s_mov_b32 s42, 0\n\t"
    "1:\n\t"
    "v_mfma_f32_32x32x16_bf16 v[184:199], v[48:51], v[32:35], 0\n\t"
    "ds_read_b128 v[44:47], v216 offset:4992\n\t"
    "v_exp_f32 v88, v152\n\t"
    "v_exp_f32 v89, v153\n\t"
    "v_cvt_pk_bf16_f32 v78, v100, v101\n\t"
    "v_exp_f32 v90, v154\n\t"
    "v_cvt_pk_bf16_f32 v79, v102, v103\n\t"
    "v_exp_f32 v91, v155\n\t"
    "v_exp_f32 v92, v156\n\t"
    "v_exp_f32 v93, v157\n\t"
    "v_mfma_f32_32x32x16_bf16 v[200:215], v[48:51], v[36:39], 0\n\t"
    "ds_read_b128 v[64:67], v218 offset:608\n\t"
    "v_exp_f32 v104, v168\n\t"
    "v_exp_f32 v105, v169\n\t"
    "v_cvt_pk_bf16_f32 v86, v116, v117\n\t"
    "v_exp_f32 v106, v170\n\t"
    "v_cvt_pk_bf16_f32 v87, v118, v119\n\t"
    "v_exp_f32 v107, v171\n\t"
    "v_exp_f32 v108, v172\n\t"
    "v_exp_f32 v109, v173\n\t"
    "v_mfma_f32_32x32x16_bf16 v[120:135], v[68:71], v[76:79], v[120:135]\n\t"
    "ds_read_b64 v[42:43], v217 offset:4992\n\t"
    "v_cvt_pk_bf16_f32 v72, v88, v89\n\t"
    "v_exp_f32 v94, v158\n\t"
    "v_cvt_pk_bf16_f32 v73, v90, v91\n\t"
    "v_exp_f32 v95, v159\n\t"
    "v_exp_f32 v96, v160\n\t"
    "v_mfma_f32_32x32x16_bf16 v[136:151], v[68:71], v[84:87], v[136:151]\n\t"
    "ds_read_b128 v[60:63], v218 offset:2496\n\t"
    "v_cvt_pk_bf16_f32 v80, v104, v105\n\t"
    "v_exp_f32 v110, v174\n\t"
    "v_cvt_pk_bf16_f32 v81, v106, v107\n\t"
    "v_exp_f32 v111, v175\n\t"
    "v_exp_f32 v112, v176\n\t"
    "v_mfma_f32_32x32x4bf16_1k v[184:215], v[52:53], v[40:41], v[184:215]\n\t"
    "s_add_u32 s42, s42, 1\n\t"
    "v_exp_f32 v97, v161\n\t"
    "v_cvt_pk_bf16_f32 v74, v92, v93\n\t"
    "v_exp_f32 v98, v162\n\t"
    "v_cvt_pk_bf16_f32 v75, v94, v95\n\t"
    "v_exp_f32 v99, v163\n\t"
    "v_exp_f32 v100, v164\n\t"
    "v_exp_f32 v113, v177\n\t"
    "v_cvt_pk_bf16_f32 v82, v108, v109\n\t"
    "v_exp_f32 v114, v178\n\t"
    "v_cvt_pk_bf16_f32 v83, v110, v111\n\t"
    "v_exp_f32 v115, v179\n\t"
    "v_exp_f32 v116, v180\n\t"
    "v_mfma_f32_32x32x16_bf16 v[120:135], v[56:59], v[72:75], v[120:135]\n\t"
    "v_exp_f32 v101, v165\n\t"
    "v_cvt_pk_bf16_f32 v76, v96, v97\n\t"
    "v_exp_f32 v102, v166\n\t"
    "v_cvt_pk_bf16_f32 v77, v98, v99\n\t"
    "v_exp_f32 v103, v167\n\t"
    "v_mfma_f32_32x32x16_bf16 v[136:151], v[56:59], v[80:83], v[136:151]\n\t"
    "s_waitcnt lgkmcnt(0)\n\t"
    "v_exp_f32 v117, v181\n\t"
    "v_cvt_pk_bf16_f32 v84, v112, v113\n\t"
    "v_exp_f32 v118, v182\n\t"
    "v_cvt_pk_bf16_f32 v85, v114, v115\n\t"
    "v_exp_f32 v119, v183\n\t"
    "v_mfma_f32_32x32x16_bf16 v[152:167], v[44:47], v[32:35], 0\n\t"
    "ds_read_b128 v[48:51], v216 offset:7488\n\t"
    "v_exp_f32 v88, v184\n\t"
    "v_exp_f32 v89, v185\n\t"
    "v_cvt_pk_bf16_f32 v78, v100, v101\n\t"
    "v_exp_f32 v90, v186\n\t"
    "v_cvt_pk_bf16_f32 v79, v102, v103\n\t"
    "v_exp_f32 v91, v187\n\t"
    "v_exp_f32 v92, v188\n\t"
    "v_exp_f32 v93, v189\n\t"
    "v_mfma_f32_32x32x16_bf16 v[168:183], v[44:47], v[36:39], 0\n\t"
    "ds_read_b128 v[68:71], v218 offset:3104\n\t"
    "v_exp_f32 v104, v200\n\t"
    "v_exp_f32 v105, v201\n\t"
    "v_cvt_pk_bf16_f32 v86, v116, v117\n\t"
    "v_exp_f32 v106, v202\n\t"
    "v_cvt_pk_bf16_f32 v87, v118, v119\n\t"
    "v_exp_f32 v107, v203\n\t"
    "v_exp_f32 v108, v204\n\t"
    "v_exp_f32 v109, v205\n\t"
    "v_mfma_f32_32x32x16_bf16 v[120:135], v[64:67], v[76:79], v[120:135]\n\t"
    "ds_read_b64 v[52:53], v217 offset:7488\n\t"
    "v_cvt_pk_bf16_f32 v72, v88, v89\n\t"
    "v_exp_f32 v94, v190\n\t"
    "v_cvt_pk_bf16_f32 v73, v90, v91\n\t"
    "v_exp_f32 v95, v191\n\t"
    "v_exp_f32 v96, v192\n\t"
    "v_mfma_f32_32x32x16_bf16 v[136:151], v[64:67], v[84:87], v[136:151]\n\t"
    "ds_read_b128 v[56:59], v218 offset:4992\n\t"
    "v_cvt_pk_bf16_f32 v80, v104, v105\n\t"
    "v_exp_f32 v110, v206\n\t"
    "v_cvt_pk_bf16_f32 v81, v106, v107\n\t"
    "v_exp_f32 v111, v207\n\t"
    "v_exp_f32 v112, v208\n\t"
    "v_mfma_f32_32x32x4bf16_1k v[152:183], v[42:43], v[40:41], v[152:183]\n\t"
    "v_exp_f32 v97, v193\n\t"
    "v_cvt_pk_bf16_f32 v74, v92, v93\n\t"
    "v_exp_f32 v98, v194\n\t"
    "v_cvt_pk_bf16_f32 v75, v94, v95\n\t"
    "v_exp_f32 v99, v195\n\t"
    "v_exp_f32 v100, v196\n\t"
    "v_exp_f32 v113, v209\n\t"
    "v_cvt_pk_bf16_f32 v82, v108, v109\n\t"
    "v_exp_f32 v114, v210\n\t"
    "v_cvt_pk_bf16_f32 v83, v110, v111\n\t"
    "v_exp_f32 v115, v211\n\t"
    "v_exp_f32 v116, v212\n\t"
    "v_mfma_f32_32x32x16_bf16 v[120:135], v[60:63], v[72:75], v[120:135]\n\t"
    "v_exp_f32 v101, v197\n\t"
    "v_cvt_pk_bf16_f32 v76, v96, v97\n\t"
    "v_exp_f32 v102, v198\n\t"
    "v_cvt_pk_bf16_f32 v77, v98, v99\n\t"
    "v_exp_f32 v103, v199\n\t"
    "v_mfma_f32_32x32x16_bf16 v[136:151], v[60:63], v[80:83], v[136:151]\n\t"
    "s_waitcnt lgkmcnt(0)\n\t"
    "v_exp_f32 v117, v213\n\t"
    "v_cvt_pk_bf16_f32 v84, v112, v113\n\t"
    "v_exp_f32 v118, v214\n\t"
    "v_cvt_pk_bf16_f32 v85, v114, v115\n\t"
    "v_exp_f32 v119, v215\n\t"
    "v_mfma_f32_32x32x16_bf16 v[184:199], v[48:51], v[32:35], 0\n\t"
    "ds_read_b128 v[44:47], v216 offset:9984\n\t"
    "v_exp_f32 v88, v152\n\t"
    "v_exp_f32 v89, v153\n\t"
    "v_cvt_pk_bf16_f32 v78, v100, v101\n\t"
    "v_exp_f32 v90, v154\n\t"
    "v_cvt_pk_bf16_f32 v79, v102, v103\n\t"
    "v_exp_f32 v91, v155\n\t"
    "v_exp_f32 v92, v156\n\t"
    "v_exp_f32 v93, v157\n\t"
    "v_mfma_f32_32x32x16_bf16 v[200:215], v[48:51], v[36:39], 0\n\t"
    "ds_read_b128 v[64:67], v218 offset:5600\n\t"
    "v_exp_f32 v104, v168\n\t"
    "v_exp_f32 v105, v169\n\t"
    "v_cvt_pk_bf16_f32 v86, v116, v117\n\t"
    "v_exp_f32 v106, v170\n\t"
    "v_cvt_pk_bf16_f32 v87, v118, v119\n\t"
    "v_exp_f32 v107, v171\n\t"
    "v_exp_f32 v108, v172\n\t"
    "v_exp_f32 v109, v173\n\t"
    "v_mfma_f32_32x32x16_bf16 v[120:135], v[68:71], v[76:79], v[120:135]\n\t"
    "ds_read_b64 v[42:43], v217 offset:9984\n\t"
    "v_cvt_pk_bf16_f32 v72, v88, v89\n\t"
    "v_exp_f32 v94, v158\n\t"
    "v_cvt_pk_bf16_f32 v73, v90, v91\n\t"
    "v_exp_f32 v95, v159\n\t"
    "v_exp_f32 v96, v160\n\t"
    "v_mfma_f32_32x32x16_bf16 v[136:151], v[68:71], v[84:87], v[136:151]\n\t"
    "ds_read_b128 v[60:63], v218 offset:7488\n\t"
    "v_cvt_pk_bf16_f32 v80, v104, v105\n\t"
    "v_exp_f32 v110, v174\n\t"
    "v_cvt_pk_bf16_f32 v81, v106, v107\n\t"
    "v_exp_f32 v111, v175\n\t"
    "v_exp_f32 v112, v176\n\t"
    "v_mfma_f32_32x32x4bf16_1k v[184:215], v[52:53], v[40:41], v[184:215]\n\t"
    "v_exp_f32 v97, v161\n\t"
    "v_cvt_pk_bf16_f32 v74, v92, v93\n\t"
    "v_exp_f32 v98, v162\n\t"
    "v_cvt_pk_bf16_f32 v75, v94, v95\n\t"
    "v_exp_f32 v99, v163\n\t"
    "v_exp_f32 v100, v164\n\t"
    "v_exp_f32 v113, v177\n\t"
    "v_cvt_pk_bf16_f32 v82, v108, v109\n\t"
    "v_exp_f32 v114, v178\n\t"
    "v_cvt_pk_bf16_f32 v83, v110, v111\n\t"
    "v_exp_f32 v115, v179\n\t"
    "v_exp_f32 v116, v180\n\t"
    "v_mfma_f32_32x32x16_bf16 v[120:135], v[56:59], v[72:75], v[120:135]\n\t"
    "v_exp_f32 v101, v165\n\t"
    "v_cvt_pk_bf16_f32 v76, v96, v97\n\t"
    "v_exp_f32 v102, v166\n\t"
    "v_cvt_pk_bf16_f32 v77, v98, v99\n\t"
    "v_exp_f32 v103, v167\n\t"
    "v_mfma_f32_32x32x16_bf16 v[136:151], v[56:59], v[80:83], v[136:151]\n\t"
    "s_waitcnt lgkmcnt(0)\n\t"
    "v_exp_f32 v117, v181\n\t"
    "v_cvt_pk_bf16_f32 v84, v112, v113\n\t"
    "v_exp_f32 v118, v182\n\t"
    "v_cvt_pk_bf16_f32 v85, v114, v115\n\t"
    "v_exp_f32 v119, v183\n\t"
    "v_mfma_f32_32x32x16_bf16 v[152:167], v[44:47], v[32:35], 0\n\t"
    "ds_read_b128 v[48:51], v216 offset:12480\n\t"
    "v_exp_f32 v88, v184\n\t"
    "v_exp_f32 v89, v185\n\t"
    "v_cvt_pk_bf16_f32 v78, v100, v101\n\t"
    "v_exp_f32 v90, v186\n\t"
    "v_cvt_pk_bf16_f32 v79, v102, v103\n\t"
    "v_exp_f32 v91, v187\n\t"
    "v_exp_f32 v92, v188\n\t"
    "v_exp_f32 v93, v189\n\t"
    "v_mfma_f32_32x32x16_bf16 v[168:183], v[44:47], v[36:39], 0\n\t"
    "ds_read_b128 v[68:71], v218 offset:8096\n\t"
    "v_exp_f32 v104, v200\n\t"
    "v_exp_f32 v105, v201\n\t"
    "v_cvt_pk_bf16_f32 v86, v116, v117\n\t"
    "v_exp_f32 v106, v202\n\t"
    "v_cvt_pk_bf16_f32 v87, v118, v119\n\t"
    "v_exp_f32 v107, v203\n\t"
    "v_exp_f32 v108, v204\n\t"
    "v_exp_f32 v109, v205\n\t"
    "v_mfma_f32_32x32x16_bf16 v[120:135], v[64:67], v[76:79], v[120:135]\n\t"
    "ds_read_b64 v[52:53], v217 offset:12480\n\t"
    "v_cvt_pk_bf16_f32 v72, v88, v89\n\t"
    "v_exp_f32 v94, v190\n\t"
    "v_cvt_pk_bf16_f32 v73, v90, v91\n\t"
    "v_exp_f32 v95, v191\n\t"
    "v_exp_f32 v96, v192\n\t"
    "v_mfma_f32_32x32x16_bf16 v[136:151], v[64:67], v[84:87], v[136:151]\n\t"
    "ds_read_b128 v[56:59], v218 offset:9984\n\t"
    "v_cvt_pk_bf16_f32 v80, v104, v105\n\t"
    "v_exp_f32 v110, v206\n\t"
    "v_cvt_pk_bf16_f32 v81, v106, v107\n\t"
    "v_exp_f32 v111, v207\n\t"
    "v_exp_f32 v112, v208\n\t"
    "v_mfma_f32_32x32x4bf16_1k v[152:183], v[42:43], v[40:41], v[152:183]\n\t"
    "s_cmp_eq_u32 s42, 1\n\t"
    "v_exp_f32 v97, v193\n\t"
    "v_cvt_pk_bf16_f32 v74, v92, v93\n\t"
    "v_exp_f32 v98, v194\n\t"
    "v_cvt_pk_bf16_f32 v75, v94, v95\n\t"
    "v_exp_f32 v99, v195\n\t"
    "v_exp_f32 v100, v196\n\t"
    "v_exp_f32 v113, v209\n\t"
    "v_cvt_pk_bf16_f32 v82, v108, v109\n\t"
    "v_exp_f32 v114, v210\n\t"
    "v_cvt_pk_bf16_f32 v83, v110, v111\n\t"
    "v_exp_f32 v115, v211\n\t"
    "v_exp_f32 v116, v212\n\t"
    "v_mfma_f32_32x32x16_bf16 v[120:135], v[60:63], v[72:75], v[120:135]\n\t"
    "v_exp_f32 v101, v197\n\t"
    "v_cvt_pk_bf16_f32 v76, v96, v97\n\t"
    "v_exp_f32 v102, v198\n\t"
    "v_cvt_pk_bf16_f32 v77, v98, v99\n\t"
    "v_exp_f32 v103, v199\n\t"
    "v_mfma_f32_32x32x16_bf16 v[136:151], v[60:63], v[80:83], v[136:151]\n\t"
    "s_waitcnt lgkmcnt(0)\n\t"
    "v_exp_f32 v117, v213\n\t"
    "v_cvt_pk_bf16_f32 v84, v112, v113\n\t"
    "v_exp_f32 v118, v214\n\t"
    "v_cvt_pk_bf16_f32 v85, v114, v115\n\t"
    "v_exp_f32 v119, v215\n\t"
    "s_cbranch_scc1 4f\n\t"
    "2:\n\t"
    "v_mfma_f32_32x32x16_bf16 v[184:199], v[48:51], v[32:35], 0\n\t"
    "ds_read_b128 v[44:47], v216 offset:14976\n\t"
    "v_exp_f32 v88, v152\n\t"
    "v_exp_f32 v89, v153\n\t"
    "v_cvt_pk_bf16_f32 v78, v100, v101\n\t"
    "v_exp_f32 v90, v154\n\t"
    "v_cvt_pk_bf16_f32 v79, v102, v103\n\t"
    "v_exp_f32 v91, v155\n\t"
    "v_exp_f32 v92, v156\n\t"
    "v_exp_f32 v93, v157\n\t"
    "v_mfma_f32_32x32x16_bf16 v[200:215], v[48:51], v[36:39], 0\n\t"
    "ds_read_b128 v[64:67], v218 offset:10592\n\t"
    "v_exp_f32 v104, v168\n\t"
    "v_exp_f32 v105, v169\n\t"
    "v_cvt_pk_bf16_f32 v86, v116, v117\n\t"
    "v_exp_f32 v106, v170\n\t"
    "v_cvt_pk_bf16_f32 v87, v118, v119\n\t"
    "v_exp_f32 v107, v171\n\t"
    "v_exp_f32 v108, v172\n\t"
    "v_exp_f32 v109, v173\n\t"
    "v_mfma_f32_32x32x16_bf16 v[120:135], v[68:71], v[76:79], v[120:135]\n\t"
    "ds_read_b64 v[42:43], v217 offset:14976\n\t"
    "v_cvt_pk_bf16_f32 v72, v88, v89\n\t"
    "v_exp_f32 v94, v158\n\t"
    "v_cvt_pk_bf16_f32 v73, v90, v91\n\t"
    "v_exp_f32 v95, v159\n\t"
    "v_exp_f32 v96, v160\n\t"
    "v_mfma_f32_32x32x16_bf16 v[136:151], v[68:71], v[84:87], v[136:151]\n\t"
    "ds_read_b128 v[60:63], v218 offset:12480\n\t"
    "v_cvt_pk_bf16_f32 v80, v104, v105\n\t"
    "v_exp_f32 v110, v174\n\t"
    "v_cvt_pk_bf16_f32 v81, v106, v107\n\t"
    "v_exp_f32 v111, v175\n\t"
    "v_exp_f32 v112, v176\n\t"
    "v_mfma_f32_32x32x4bf16_1k v[184:215], v[52:53], v[40:41], v[184:215]\n\t"
    "v_exp_f32 v97, v161\n\t"
    "v_cvt_pk_bf16_f32 v74, v92, v93\n\t"
    "v_exp_f32 v98, v162\n\t"
    "v_cvt_pk_bf16_f32 v75, v94, v95\n\t"
    "v_exp_f32 v99, v163\n\t"
    "v_exp_f32 v100, v164\n\t"
    "v_exp_f32 v113, v177\n\t"
    "v_cvt_pk_bf16_f32 v82, v108, v109\n\t"
    "v_exp_f32 v114, v178\n\t"
    "v_cvt_pk_bf16_f32 v83, v110, v111\n\t"
    "v_exp_f32 v115, v179\n\t"
    "v_exp_f32 v116, v180\n\t"
    "v_mfma_f32_32x32x16_bf16 v[120:135], v[56:59], v[72:75], v[120:135]\n\t"
    "v_exp_f32 v101, v165\n\t"
    "v_cvt_pk_bf16_f32 v76, v96, v97\n\t"
    "v_exp_f32 v102, v166\n\t"
    "v_cvt_pk_bf16_f32 v77, v98, v99\n\t"
    "v_exp_f32 v103, v167\n\t"
    "v_mfma_f32_32x32x16_bf16 v[136:151], v[56:59], v[80:83], v[136:151]\n\t"
    "s_waitcnt lgkmcnt(0)\n\t"
    "v_exp_f32 v117, v181\n\t"
    "v_cvt_pk_bf16_f32 v84, v112, v113\n\t"
    "v_exp_f32 v118, v182\n\t"
    "v_cvt_pk_bf16_f32 v85, v114, v115\n\t"
    "v_exp_f32 v119, v183\n\t"
    "v_mfma_f32_32x32x16_bf16 v[152:167], v[44:47], v[32:35], 0\n\t"
    "ds_read_b128 v[48:51], v216 offset:17472\n\t"
    "v_exp_f32 v88, v184\n\t"
    "v_exp_f32 v89, v185\n\t"
    "v_cvt_pk_bf16_f32 v78, v100, v101\n\t"
    "v_exp_f32 v90, v186\n\t"
    "v_cvt_pk_bf16_f32 v79, v102, v103\n\t"
    "v_exp_f32 v91, v187\n\t"
    "v_exp_f32 v92, v188\n\t"
    "v_exp_f32 v93, v189\n\t"
    "v_mfma_f32_32x32x16_bf16 v[168:183], v[44:47], v[36:39], 0\n\t"
    "ds_read_b128 v[68:71], v218 offset:13088\n\t"
    "v_exp_f32 v104, v200\n\t"
    "v_exp_f32 v105, v201\n\t"
    "v_cvt_pk_bf16_f32 v86, v116, v117\n\t"
    "v_exp_f32 v106, v202\n\t"
    "v_cvt_pk_bf16_f32 v87, v118, v119\n\t"
    "v_exp_f32 v107, v203\n\t"
    "v_exp_f32 v108, v204\n\t"
    "v_exp_f32 v109, v205\n\t"
    "v_mfma_f32_32x32x16_bf16 v[120:135], v[64:67], v[76:79], v[120:135]\n\t"
    "ds_read_b64 v[52:53], v217 offset:17472\n\t"
    "v_cvt_pk_bf16_f32 v72, v88, v89\n\t"
    "v_exp_f32 v94, v190\n\t"
    "v_cvt_pk_bf16_f32 v73, v90, v91\n\t"
    "v_exp_f32 v95, v191\n\t"
    "v_exp_f32 v96, v192\n\t"
    "v_mfma_f32_32x32x16_bf16 v[136:151], v[64:67], v[84:87], v[136:151]\n\t"
    "ds_read_b128 v[56:59], v218 offset:14976\n\t"
    "v_cvt_pk_bf16_f32 v80, v104, v105\n\t"
    "v_exp_f32 v110, v206\n\t"
    "v_cvt_pk_bf16_f32 v81, v106, v107\n\t"
    "v_exp_f32 v111, v207\n\t"
    "v_exp_f32 v112, v208\n\t"
    "v_mfma_f32_32x32x4bf16_1k v[152:183], v[42:43], v[40:41], v[152:183]\n\t"
    "v_exp_f32 v97, v193\n\t"
    "v_cvt_pk_bf16_f32 v74, v92, v93\n\t"
    "v_exp_f32 v98, v194\n\t"
    "v_cvt_pk_bf16_f32 v75, v94, v95\n\t"
    "v_exp_f32 v99, v195\n\t"
    "v_exp_f32 v100, v196\n\t"
    "v_exp_f32 v113, v209\n\t"
    "v_cvt_pk_bf16_f32 v82, v108, v109\n\t"
    "v_exp_f32 v114, v210\n\t"
    "v_cvt_pk_bf16_f32 v83, v110, v111\n\t"
    "v_exp_f32 v115, v211\n\t"
    "v_exp_f32 v116, v212\n\t"
    "v_mfma_f32_32x32x16_bf16 v[120:135], v[60:63], v[72:75], v[120:135]\n\t"
    "v_exp_f32 v101, v197\n\t"
    "v_cvt_pk_bf16_f32 v76, v96, v97\n\t"
    "v_exp_f32 v102, v198\n\t"
    "v_cvt_pk_bf16_f32 v77, v98, v99\n\t"
    "v_exp_f32 v103, v199\n\t"
    "v_mfma_f32_32x32x16_bf16 v[136:151], v[60:63], v[80:83], v[136:151]\n\t"
    "s_waitcnt lgkmcnt(0)\n\t"
    "v_exp_f32 v117, v213\n\t"
    "v_cvt_pk_bf16_f32 v84, v112, v113\n\t"
    "v_exp_f32 v118, v214\n\t"
    "v_cvt_pk_bf16_f32 v85, v114, v115\n\t"
    "v_exp_f32 v119, v215\n\t"
    "v_mfma_f32_32x32x16_bf16 v[184:199], v[48:51], v[32:35], 0\n\t"
    "ds_read_b128 v[44:47], v216 offset:19968\n\t"
    "v_exp_f32 v88, v152\n\t"
    "v_exp_f32 v89, v153\n\t"
    "v_cvt_pk_bf16_f32 v78, v100, v101\n\t"
    "v_exp_f32 v90, v154\n\t"
    "v_cvt_pk_bf16_f32 v79, v102, v103\n\t"
    "v_exp_f32 v91, v155\n\t"
    "v_exp_f32 v92, v156\n\t"
    "v_exp_f32 v93, v157\n\t"
    "v_mfma_f32_32x32x16_bf16 v[200:215], v[48:51], v[36:39], 0\n\t"
    "ds_read_b128 v[64:67], v218 offset:15584\n\t"
    "v_exp_f32 v104, v168\n\t"
    "v_exp_f32 v105, v169\n\t"
    "v_cvt_pk_bf16_f32 v86, v116, v117\n\t"
    "v_exp_f32 v106, v170\n\t"
    "v_cvt_pk_bf16_f32 v87, v118, v119\n\t"
    "v_exp_f32 v107, v171\n\t"
    "v_exp_f32 v108, v172\n\t"
    "v_exp_f32 v109, v173\n\t"
    "v_mfma_f32_32x32x16_bf16 v[120:135], v[68:71], v[76:79], v[120:135]\n\t"
    "ds_read_b64 v[42:43], v217 offset:19968\n\t"
    "v_cvt_pk_bf16_f32 v72, v88, v89\n\t"
    "v_exp_f32 v94, v158\n\t"
    "v_cvt_pk_bf16_f32 v73, v90, v91\n\t"
    "v_exp_f32 v95, v159\n\t"
    "v_exp_f32 v96, v160\n\t"
    "v_mfma_f32_32x32x16_bf16 v[136:151], v[68:71], v[84:87], v[136:151]\n\t"
    "ds_read_b128 v[60:63], v218 offset:17472\n\t"
    "v_cvt_pk_bf16_f32 v80, v104, v105\n\t"
    "v_exp_f32 v110, v174\n\t"
    "v_cvt_pk_bf16_f32 v81, v106, v107\n\t"
    "v_exp_f32 v111, v175\n\t"
    "v_exp_f32 v112, v176\n\t"
    "v_mfma_f32_32x32x4bf16_1k v[184:215], v[52:53], v[40:41], v[184:215]\n\t"
    "ds_read_b32 v220, v219 offset:12\n\t"
    "v_exp_f32 v97, v161\n\t"
    "v_cvt_pk_bf16_f32 v74, v92, v93\n\t"
    "v_exp_f32 v98, v162\n\t"
    "v_cvt_pk_bf16_f32 v75, v94, v95\n\t"
    "v_exp_f32 v99, v163\n\t"
    "v_exp_f32 v100, v164\n\t"
    "v_exp_f32 v113, v177\n\t"
    "v_cvt_pk_bf16_f32 v82, v108, v109\n\t"
    "v_exp_f32 v114, v178\n\t"
    "v_cvt_pk_bf16_f32 v83, v110, v111\n\t"
    "v_exp_f32 v115, v179\n\t"
    "v_exp_f32 v116, v180\n\t"
    "v_mfma_f32_32x32x16_bf16 v[120:135], v[56:59], v[72:75], v[120:135]\n\t"
    "v_exp_f32 v101, v165\n\t"
    "v_cvt_pk_bf16_f32 v76, v96, v97\n\t"
    "v_exp_f32 v102, v166\n\t"
    "v_cvt_pk_bf16_f32 v77, v98, v99\n\t"
    "v_exp_f32 v103, v167\n\t"
    "v_mfma_f32_32x32x16_bf16 v[136:151], v[56:59], v[80:83], v[136:151]\n\t"
    "s_waitcnt lgkmcnt(0)\n\t"
    "v_exp_f32 v117, v181\n\t"
    "v_cvt_pk_bf16_f32 v84, v112, v113\n\t"
    "v_exp_f32 v118, v182\n\t"
    "v_cvt_pk_bf16_f32 v85, v114, v115\n\t"
    "v_exp_f32 v119, v183\n\t"
    "v_mfma_f32_32x32x16_bf16 v[152:167], v[44:47], v[32:35], 0\n\t"
    "ds_read_b128 v[48:51], v216 offset:22464\n\t"
    "v_exp_f32 v88, v184\n\t"
    "v_exp_f32 v89, v185\n\t"
    "v_cvt_pk_bf16_f32 v78, v100, v101\n\t"
    "v_exp_f32 v90, v186\n\t"
    "v_cvt_pk_bf16_f32 v79, v102, v103\n\t"
    "v_exp_f32 v91, v187\n\t"
    "v_exp_f32 v92, v188\n\t"
    "v_exp_f32 v93, v189\n\t"
    "v_mfma_f32_32x32x16_bf16 v[168:183], v[44:47], v[36:39], 0\n\t"
    "ds_read_b128 v[68:71], v218 offset:18080\n\t"
    "v_exp_f32 v104, v200\n\t"
    "v_exp_f32 v105, v201\n\t"
    "v_cvt_pk_bf16_f32 v86, v116, v117\n\t"
    "v_exp_f32 v106, v202\n\t"
    "v_cvt_pk_bf16_f32 v87, v118, v119\n\t"
    "v_exp_f32 v107, v203\n\t"
    "v_exp_f32 v108, v204\n\t"
    "v_exp_f32 v109, v205\n\t"
    "v_mfma_f32_32x32x16_bf16 v[120:135], v[64:67], v[76:79], v[120:135]\n\t"
    "ds_read_b64 v[52:53], v217 offset:22464\n\t"
    "v_cvt_pk_bf16_f32 v72, v88, v89\n\t"
    "v_exp_f32 v94, v190\n\t"
    "v_cvt_pk_bf16_f32 v73, v90, v91\n\t"
    "v_exp_f32 v95, v191\n\t"
    "v_exp_f32 v96, v192\n\t"
    "v_mfma_f32_32x32x16_bf16 v[136:151], v[64:67], v[84:87], v[136:151]\n\t"
    "ds_read_b128 v[56:59], v218 offset:19968\n\t"
    "v_cvt_pk_bf16_f32 v80, v104, v105\n\t"
    "v_exp_f32 v110, v206\n\t"
    "v_cvt_pk_bf16_f32 v81, v106, v107\n\t"
    "v_exp_f32 v111, v207\n\t"
    "v_exp_f32 v112, v208\n\t"
    "v_mfma_f32_32x32x4bf16_1k v[152:183], v[42:43], v[40:41], v[152:183]\n\t"
    "v_readfirstlane_b32 s43, v220\n\t"
    "v_exp_f32 v97, v193\n\t"
    "v_cvt_pk_bf16_f32 v74, v92, v93\n\t"
    "v_exp_f32 v98, v194\n\t"
    "v_cvt_pk_bf16_f32 v75, v94, v95\n\t"
    "v_exp_f32 v99, v195\n\t"
    "v_exp_f32 v100, v196\n\t"
    "v_exp_f32 v113, v209\n\t"
    "v_cvt_pk_bf16_f32 v82, v108, v109\n\t"
    "v_exp_f32 v114, v210\n\t"
    "v_cvt_pk_bf16_f32 v83, v110, v111\n\t"
    "v_exp_f32 v115, v211\n\t"
    "v_exp_f32 v116, v212\n\t"
    "v_mfma_f32_32x32x16_bf16 v[120:135], v[60:63], v[72:75], v[120:135]\n\t"
    "s_add_u32 s44, s42, 2\n\t"
    "v_exp_f32 v101, v197\n\t"
    "v_cvt_pk_bf16_f32 v76, v96, v97\n\t"
    "v_exp_f32 v102, v198\n\t"
    "v_cvt_pk_bf16_f32 v77, v98, v99\n\t"
    "v_exp_f32 v103, v199\n\t"
    "v_mfma_f32_32x32x16_bf16 v[136:151], v[60:63], v[80:83], v[136:151]\n\t"
    "s_waitcnt lgkmcnt(0)\n\t"
    "s_cmp_lt_u32 s43, s44\n\t"
    "v_exp_f32 v117, v213\n\t"
    "v_cvt_pk_bf16_f32 v84, v112, v113\n\t"
    "v_exp_f32 v118, v214\n\t"
    "v_cvt_pk_bf16_f32 v85, v114, v115\n\t"
    "v_exp_f32 v119, v215\n\t"
    "v_add_u32 v216, 19968, v216\n\t"
    "v_add_u32 v217, 19968, v217\n\t"
    "v_add_u32 v218, 19968, v218\n\t"
    "s_cbranch_scc1 5f\n\t"
    "3:\n\t"
    "s_cmp_lt_u32 s42, 8\n\t"
    "s_cbranch_scc1 1b\n\t"
    "s_branch 6f\n\t"
    "4:\n\t"
    "8:\n\t"
    "ds_read_b128 v[220:223], v219\n\t"
    "s_waitcnt lgkmcnt(0)\n\t"
    "v_min_u32 v220, v220, v221\n\t"
    "v_min3_u32 v220, v220, v222, v223\n\t"
    "s_nop 0\n\t"
    "v_readfirstlane_b32 s43, v220\n\t"
    "s_nop 3\n\t"
    "s_cmp_ge_u32 s43, 2\n\t"
    "s_cbranch_scc0 8b\n\t"
    "s_branch 2b\n\t"
    "5:\n\t"
    "s_cmp_ge_u32 s42, 8\n\t"
    "s_cbranch_scc1 6f\n\t"
    "9:\n\t"
    "ds_read_b32 v220, v219 offset:12\n\t"
    "s_waitcnt lgkmcnt(0)\n\t"
    "v_readfirstlane_b32 s43, v220\n\t"
    "s_nop 3\n\t"
    "s_cmp_ge_u32 s43, s44\n\t"
    "s_cbranch_scc1 3b\n\t"
    "s_sleep 1\n\t"
    "s_branch 9b\n\t"
    "6:\n\t"
    "s_nop 0\n\t"
    "v_cvt_pk_bf16_f32 v78, v100, v101\n\t"
    "v_cvt_pk_bf16_f32 v79, v102, v103\n\t"
    "v_cvt_pk_bf16_f32 v86, v116, v117\n\t"
    "v_cvt_pk_bf16_f32 v87, v118, v119\n\t"
    "s_waitcnt lgkmcnt(0)\n\t"
    "s_nop 1\n\t"
    "v_mfma_f32_32x32x16_bf16 v[120:135], v[68:71], v[76:79], v[120:135]\n\t"
    "v_mfma_f32_32x32x16_bf16 v[136:151], v[68:71], v[84:87], v[136:151]\n\t"
    "s_nop 7\n\t"
    "s_nop 7\n\t"
    "s_setprio 0\n\t"
      "v_mov_b32 %[a0], v120\n\tv_mov_b32 %[a1], v121\n\tv_mov_b32 %[a2], v122\n\tv_mov_b32 %[a3], v123\n\t"
      "v_mov_b32 %[a4], v124\n\tv_mov_b32 %[a5], v125\n\tv_mov_b32 %[a6], v126\n\tv_mov_b32 %[a7], v127\n\t"
      "v_mov_b32 %[a8], v128\n\tv_mov_b32 %[a9], v129\n\tv_mov_b32 %[a10], v130\n\t"
      "v_mov_b32 %[b0], v136\n\tv_mov_b32 %[b1], v137\n\tv_mov_b32 %[b2], v138\n\tv_mov_b32 %[b3], v139\n\t"
      "v_mov_b32 %[b4], v140\n\tv_mov_b32 %[b5], v141\n\tv_mov_b32 %[b6], v142\n\tv_mov_b32 %[b7], v143\n\t"
      "v_mov_b32 %[b8], v144\n\tv_mov_b32 %[b9], v145\n\tv_mov_b32 %[b10], v146"
      : [a0] "=&v"(oa[0]), [a1] "=&v"(oa[1]), [a2] "=&v"(oa[2]), [a3] "=&v"(oa[3]), [a4] "=&v"(oa[4]), [a5] "=&v"(oa[5]), [a6] "=&v"(oa[6]), [a7] "=&v"(oa[7]), [a8] "=&v"(oa[8]), [a9] "=&v"(oa[9]), [a10] "=&v"(oa[10]),
        [b0] "=&v"(ob[0]), [b1] "=&v"(ob[1]), [b2] "=&v"(ob[2]), [b3] "=&v"(ob[3]), [b4] "=&v"(ob[4]), [b5] "=&v"(ob[5]), [b6] "=&v"(ob[6]), [b7] "=&v"(ob[7]), [b8] "=&v"(ob[8]), [b9] "=&v"(ob[9]), [b10] "=&v"(ob[10])
      : [qa0] "v"(qa[0]), [qa1] "v"(qa[1]), [qa2] "v"(qa[2]), [qa3] "v"(qa[3]), [qb0] "v"(qb[0]), [qb1] "v"(qb[1]), [qb2] "v"(qb[2]), [qb3] "v"(qb[3]),
        [qr0] "v"(qr), [qr1] "v"(mb), [aK] "v"(aK), [aR] "v"(aR), [aV] "v"(aV), [aF] "v"(LDS_FLAG)
      : "v32", "v33", "v34", "v35", "v36", "v37", "v38", "v39", "v40", "v41", "v42", "v43", "v44", "v45", "v46", "v47", "v48", "v49", "v50", "v51", "v52", "v53", "v54", "v55", "v56", "v57", "v58", "v59", "v60", "v61", "v62", "v63", "v64", "v65", "v66", "v67", "v68", "v69", "v70", "v71", "v72", "v73", "v74", "v75", "v76", "v77", "v78", "v79", "v80", "v81", "v82", "v83", "v84", "v85", "v86", "v87", "v88", "v89", "v90", "v91", "v92", "v93", "v94", "v95", "v96", "v97", "v98", "v99", "v100", "v101", "v102", "v103", "v104", "v105", "v106", "v107", "v108", "v109", "v110", "v111", "v112", "v113", "v114", "v115", "v116", "v117", "v118", "v119", "v120", "v121", "v122", "v123", "v124", "v125", "v126", "v127", "v128", "v129", "v130", "v131", "v132", "v133", "v134", "v135", "v136", "v137", "v138", "v139", "v140", "v141", "v142", "v143", "v144", "v145", "v146", "v147", "v148", "v149", "v150", "v151", "v152", "v153", "v154", "v155", "v156", "v157", "v158", "v159", "v160", "v161", "v162", "v163", "v164", "v165", "v166", "v167", "v168", "v169", "v170", "v171", "v172", "v173", "v174", "v175", "v176", "v177", "v178", "v179", "v180", "v181", "v182", "v183", "v184", "v185", "v186", "v187", "v188", "v189", "v190", "v191", "v192", "v193", "v194", "v195", "v196", "v197", "v198", "v199", "v200", "v201", "v202", "v203", "v204", "v205", "v206", "v207", "v208", "v209", "v210", "v211", "v212", "v213", "v214", "v215", "v216", "v217", "v218", "v219", "v220", "v221", "v222", "v223", "v224", "s42", "s43", "s44", "scc", "memory");
  }
  char* orow = ws + WS_O + tokA * ROWB + r * 36;
  {
    const float inv = __builtin_amdgcn_rcpf(oa[10]);
    *(u32x4*)(orow + 16 * h) = u32x4{pk2(oa[0] * inv, oa[1] * inv), pk2(oa[2] * inv, oa[3] * inv), pk2(oa[4] * inv, oa[5] * inv), pk2(oa[6] * inv, oa[7] * inv)};
    if (h == 0) *(unsigned*)(orow + 32) = pk2(oa[8] * inv, oa[9] * inv);
  }
  {
    const float inv = __builtin_amdgcn_rcpf(ob[10]);
    *(u32x4*)(orow + ROWB + 16 * h) = u32x4{pk2(ob[0] * inv, ob[1] * inv), pk2(ob[2] * inv, ob[3] * inv), pk2(ob[4] * inv, ob[5] * inv), pk2(ob[6] * inv, ob[7] * inv)};
    if (h == 0) *(unsigned*)(orow + ROWB + 32) = pk2(ob[8] * inv, ob[9] * inv);
  }
}

__global__ void __launch_bounds__(512, 2) k_post(const char* __restrict__ ws, const char* __restrict__ wo, char* __restrict__ out) {
  extern __shared__ __attribute__((aligned(16))) char smem[];
  LAS char* lds = (LAS char*)smem;
  const int lane = threadIdx.x & 63, w = __builtin_amdgcn_readfirstlane(threadIdx.x >> 6);
  asm volatile("lbl_post_begin:");
  asm volatile("" :: "s"(ws), "s"(wo), "s"(out));
  const int xcd = blockIdx.x & 7, y = blockIdx.x >> 3;
  const bool proj = SPEC ? xcd < 5 : y < 20;
  const int q = SPEC ? (proj ? xcd : xcd - 5) * 32 + y : (proj ? xcd * 20 + y : xcd * 12 + y - 20), rb = proj ? q / 5 : q / 3, c = proj ? q % 5 : q % 3;
  if (__builtin_expect(w >= 4, 1)) {
    Stager g;
    stager_init(g, w - 4, lane, proj ? ws + WS_O + rb * 64 * ROWB : ws + WS_T2 + 2 * rb * 32 * FP * 4, proj ? 32 * ROWB : 32 * FP * 4, proj ? ROWB : FP * 4,
                proj ? wo + c * 256 : P_INV + c * 108 * 1024, !proj, false, proj && c == 4 ? 7 : 15, wo, 0, 0, 0);
    if (proj) gemm_producer<4>(g
      );
    else gemm_producer<3>(g
      );
    return;
  }
  const int r = lane & 15, kq = lane >> 4;
  if (proj) {
    const int wm = w & 1, wn = w >> 1;
    f32x4 acc[4][2];
    gemm_consumer_w<0>(lds, lane, 32 * wm, 4 * wn, acc
        );
    if (c < 4 || wn == 0) store_tile<WT_OUT>(lds, lane, w, acc, 1.0f, out + (size_t)(rb * 64 + 32 * wm) * (2 * ROWB) + c * 256 + wn * 128, 2 * ROWB);
    asm volatile("lbl_post_end2:");
  } else {
    const int gq = w & 1, par = w >> 1, n2 = 2 * rb + gq;
    u32x4 a288[2] = {u32x4{0, 0, 0, 0}, u32x4{0, 0, 0, 0}};
    if (par == 0 && r == 0) {
#pragma unroll
      for (int a = 0; a < 2; ++a)
#pragma unroll
        for (int i = 0; i < 4; ++i) a288[a][i] = ((const unsigned*)(ws + WS_T2))[(n2 * 32 + 16 * a + 4 * kq + i) * FP + 288];
    }
    const float k0f = (float)(4 * kq), nf = (float)(n2 + 64 * r) * (1.0f / 2048.0f);
    u32x4 Bre[2][2], Bim[2][2];
#pragma unroll
    for (int nt = 0; nt < 2; ++nt)
#pragma unroll
      for (int a = 0; a < 2; ++a)
#pragma unroll
        for (int i = 0; i < 4; ++i) {
          float cc, ss; cs_rev((k0f + (float)(16 * a + i)) * (nf + (float)nt * 0.5f), cc, ss);
          Bre[nt][a][i] = pk2(cc, -ss); Bim[nt][a][i] = pk2(ss, cc);
        }
    asm volatile("" : "+v"(Bre[0][0]), "+v"(Bre[0][1]), "+v"(Bre[1][0]), "+v"(Bre[1][1]), "+v"(Bim[0][0]), "+v"(Bim[0][1]), "+v"(Bim[1][0]), "+v"(Bim[1][1]));
    f32x4 acc[6][2];
#pragma unroll
    for (int j = 0; j < 6; ++j) { acc[j][0] = f32x4{0, 0, 0, 0}; acc[j][1] = f32x4{0, 0, 0, 0}; }
    unsigned ax[2][4];
#pragma unroll
    for (int a = 0; a < 2; ++a)
#pragma unroll
      for (int i = 0; i < 4; ++i) {
        ax[a][i] = LDS_A + (32 * gq + 16 * a + 4 * kq + i) * 128 + (((r >> 1) ^ (4 * (kq & 1) + i)) << 4) + 8 * (r & 1) + 4 * par;
        asm volatile("" : "+v"(ax[a][i]));
      }
    LAS char* tb = lds + LDS_B + par * 6144 + lane * 16;
    struct FF { u32x4 a[2], t[6], zb[2]; } f0, f1, f2;
    auto L = [&](FF& d, int s, int slot) {
#pragma unroll
      for (int j = 0; j < 6; ++j) d.t[j] = *(LAS u32x4*)(tb + slot * SLOT_B + j * 1024);
#pragma unroll
      for (int a = 0; a < 2; ++a)
#pragma unroll
        for (int i = 0; i < 4; ++i) d.a[a][i] = *(LAS unsigned*)(lds + ax[a][i] + s * 8192);
    };
    auto Zp = [&](FF& d, const u32x4 (&ai)[2]) {
      f32x4 zr[2], zi[2];
#pragma unroll
      for (int nt = 0; nt < 2; ++nt) { zr[nt] = mfma16(d.a[0], Bre[nt][0], f32x4{0, 0, 0, 0}); zi[nt] = mfma16(ai[0], Bim[nt][0], f32x4{0, 0, 0, 0}); }
#pragma unroll
      for (int nt = 0; nt < 2; ++nt) { zr[nt] = mfma16(d.a[1], Bre[nt][1], zr[nt]); zi[nt] = mfma16(ai[1], Bim[nt][1], zi[nt]); }
      d.zb[0] = pk8(zr[0], zi[0]); d.zb[1] = pk8(zr[1], zi[1]);
    };
    auto Z = [&](FF& d) { Zp(d, d.a); };
    auto M = [&](const FF& d) {
#pragma unroll
      for (int j = 0; j < 6; ++j)
#pragma unroll
        for (int nt = 0; nt < 2; ++nt) acc[j][nt] = mfma16(d.t[j], d.zb[nt], acc[j][nt]);
    };
    int slot = 1;
    auto step = [&](FF& ld, FF& zz, const FF& mm, int s) {
      BARRIER();
      slot = next_slot(slot);
      L(ld, s, slot);
      Z(zz);
      M(mm);
      sched_interleave<14, 1>();
      SCHED_FENCE();
    };
    const bool sp = par == 0 && r == 0;
    BARRIER();
    L(f0, 0, 0);
    BARRIER();
    L(f1, 1, 1);
    {
      u32x4 ai[2];
#pragma unroll
      for (int a = 0; a < 2; ++a)
#pragma unroll
        for (int i = 0; i < 4; ++i) { const unsigned v = a288[a][i]; ai[a][i] = sp ? (((v >> 16) ^ 0x8000u) | (v << 16)) : f0.a[a][i]; }
      Zp(f0, ai);
    }
    SCHED_FENCE();
#pragma unroll 1
    for (int s = 2; s < 8; s += 3) {
      step(f2, f1, f0, s);
      step(f0, f2, f1, s + 1);
      step(f1, f0, f2, s + 2);
    }
    step(f2, f1, f0, 8);
    Z(f2);
    M(f1);
    M(f2);
    LAS char* stg = lds + par * 25600;
#pragma unroll
    for (int j = 0; j < 6; ++j)
#pragma unroll
      for (int nt = 0; nt < 2; ++nt) *(LAS f32x4*)(stg + (32 * gq + 16 * nt + r) * 400 + j * 64 + kq * 16) = acc[j][nt];
    WAIT_BARRIER();
    const float sc = 1.0f / ((float)S * (float)D);
#pragma unroll
    for (int i = 0; i < 3; ++i) {
      const int idx = i * 256 + w * 64 + lane, row = (idx * 5462) >> 16, ch = idx - row * 12;
      LAS char* pe = lds + row * 400 + ch * 32;
      const f32x4 e0 = *(LAS f32x4*)pe * sc, e1 = *(LAS f32x4*)(pe + 16) * sc, o0 = *(LAS f32x4*)(pe + 25600) * sc, o1 = *(LAS f32x4*)(pe + 25616) * sc;
      char* dst = out + (size_t)(2 * rb + (row >> 5) + 64 * (row & 31)) * (2 * ROWB) + ROWB + c * 192 + ch * 16;
      st16<WT_OUTF>(dst, pk8(e0 + o0, e1 + o1));
      st16<WT_OUTF>(dst + 576, pk8(e0 - o0, e1 - o1));
    }
    asm volatile("lbl_post_end:");
  }
}
}

extern "C" void kernel_launch(void* const* d_in, const int* in_sizes, int n_in, void* d_out, int out_size, void* d_ws, size_t ws_size, hipStream_t stream) {
  const char* ax = (const char*)d_in[0]; const char* wq = (const char*)d_in[1]; const char* wk = (const char*)d_in[2]; const char* wv = (const char*)d_in[3];
  const char* wo = (const char*)d_in[4]; const char* bx = (const char*)d_in[5]; const u16* wr = (const u16*)d_in[6]; const u16* wi = (const u16*)d_in[7];
  char* ws = (char*)d_ws;
  (void)hipFuncSetAttribute((const void*)k_pre, hipFuncAttributeMaxDynamicSharedMemorySize, GEMM_LDS);
  (void)hipFuncSetAttribute((const void*)k_mid, hipFuncAttributeMaxDynamicSharedMemorySize, IMG_B);
  (void)hipFuncSetAttribute((const void*)k_post, hipFuncAttributeMaxDynamicSharedMemorySize, GEMM_LDS);
  k_pre<<<256, 512, GEMM_LDS, stream>>>(ax, wq, wk, wv, bx, ws);
  k_mid<<<256, 512, IMG_B, stream>>>(ws, wr, wi, wo);
  k_post<<<256, 512, GEMM_LDS, stream>>>(ws, wo, (char*)d_out);
}
```
